```python
import torch

def _softmax(x, axis=-1):
    return torch.softmax(x, dim=axis)
def _gelu(x):
    return torch.nn.functional.gelu(x, approximate="tanh")
def _silu(x):
    return torch.nn.functional.silu(x)
def _sigmoid(x):
    return torch.sigmoid(x)
def _layernorm(x, axis=-1, eps=1e-5):
    m = x.mean(dim=axis, keepdim=True)
    v = x.var(dim=axis, keepdim=True, unbiased=False)
    return (x - m) / torch.sqrt(v + eps)
def _scatter_add(tgt, idx, src, axis=0):
    return tgt.clone().index_add_(axis, idx, src)
def _segment_sum(src, seg, n):
    out = torch.zeros((n,) + src.shape[1:], dtype=src.dtype)
    return out.index_add_(0, seg, src)
def _pad(x, pw):
    flat = tuple(v for pair in reversed(pw) for v in pair)
    return torch.nn.functional.pad(x, flat)

kernel_name = 'sequential_preLN__sliding_block_cached__swiglu_ffn__decode'

def setup_inputs():

    _w = (lambda a: (lambda t: t.to(torch.bfloat16) if t.is_floating_point() else t)(torch.as_tensor(a)))
    def _A_setup():
        B, Sq, D = 2, 1, 640
        Hq, Hkv, Sk, Dh = 32, 32, 4096, 128
        assert Hq % Hkv == 0, (Hq, Hkv)
        return {
            "x": _w((lambda *s: torch.randn(*s, dtype=torch.bfloat16) * 0.05)(B, Sq, D)),
            "k_cache": _w((lambda *s: torch.randn(*s, dtype=torch.bfloat16) * 3.2)(B, Hkv, Sk, Dh)),
            "v_cache": _w((lambda *s: torch.randn(*s, dtype=torch.bfloat16) * 0.4)(B, Hkv, Sk, Dh)),
            "wq": _w((lambda *s: torch.randn(*s, dtype=torch.bfloat16) * 3.2)(D, Hq * Dh) * (D ** -0.5)),
            "wo": _w((lambda *s: torch.randn(*s, dtype=torch.bfloat16) * 0.4)(Hq * Dh, D) * ((Hq * Dh) ** -0.5)),
        }
    _A_in = _A_setup()

    def _B_setup():
        B, S, D, F = 2, 1, 640, 1728
        return {
            "x": _w((lambda *s: torch.randn(*s, dtype=torch.bfloat16) * 0.05)(B, S, D)),
            "w_gate": _w((lambda *s: torch.randn(*s, dtype=torch.bfloat16) * 0.05)(D, F)),
            "w_up": _w((lambda *s: torch.randn(*s, dtype=torch.bfloat16) * 0.05)(D, F)),
            "w_down": _w((lambda *s: torch.randn(*s, dtype=torch.bfloat16) * 0.05)(F, D)),
        }
    _B_in = _B_setup()

    return {
        "A_x": _A_in["x"],
        "A_k_cache": _A_in["k_cache"],
        "A_v_cache": _A_in["v_cache"],
        "A_wq": _A_in["wq"],
        "A_wo": _A_in["wo"],
        "B_x": _B_in["x"],
        "B_w_gate": _B_in["w_gate"],
        "B_w_up": _B_in["w_up"],
        "B_w_down": _B_in["w_down"],
    }

def reference(A_x, A_k_cache, A_v_cache, A_wq, A_wo, B_x, B_w_gate, B_w_up, B_w_down):
    def _A_ref(x, k_cache, v_cache, wq, wo):
        B, Sq, D = x.shape
        Hq, Dh = 32, 128
        Hkv, Sk = k_cache.shape[1], k_cache.shape[2]
        W = min(4096, max(256, Sk // 4))
        rep = Hq // Hkv
        q = (lambda x, *d: x.permute(*d))((x @ wq).reshape(B, Sq, Hq, Dh), 0, 2, 1, 3)
        k = (lambda x, n, axis: torch.repeat_interleave(x, n, dim=axis))(k_cache[:, :, Sk - W:, :], rep, axis=1)
        v = (lambda x, n, axis: torch.repeat_interleave(x, n, dim=axis))(v_cache[:, :, Sk - W:, :], rep, axis=1)
        scores = torch.einsum("bhqd,bhkd->bhqk", q, k) * (Dh ** -0.5)
        p = _softmax(scores, axis=-1)
        o = torch.einsum("bhqk,bhkd->bhqd", p, v)
        return (lambda x, *d: x.permute(*d))(o, 0, 2, 1, 3).reshape(B, Sq, Hq * Dh) @ wo

    def _B_ref(x, w_gate, w_up, w_down):
        g = _silu(x @ w_gate)
        u = x @ w_up
        return (g * u) @ w_down

    x = A_x
    h = x + _A_ref(_layernorm(x, axis=-1), A_k_cache, A_v_cache, A_wq, A_wo)
    h2 = h + _B_ref(_layernorm(h, axis=-1), B_w_gate, B_w_up, B_w_down)
    return h2
```

```cpp
#include <hip/hip_runtime.h>
#include <hip/hip_bf16.h>
#define MIMIC 1
typedef unsigned short u16;
__device__ __forceinline__ float bf2f(u16 v) { return __uint_as_float((unsigned)v << 16); }
__device__ __forceinline__ float rbf(float f) {
  return __bfloat162float(__float2bfloat16(f));
}
constexpr int D = 640, H = 32, DH = 128, SK = 4096, W = 1024, F = 1728;

__device__ float block_sum(float v, float* red) {
  for (int o = 32; o; o >>= 1) v += __shfl_xor(v, o);
  __syncthreads();
  if ((threadIdx.x & 63) == 0) red[threadIdx.x >> 6] = v;
  __syncthreads();
  float s = 0;
  for (int i = 0; i < (int)(blockDim.x >> 6); ++i) s += red[i];
  return s;
}
__device__ float block_max(float v, float* red) {
  for (int o = 32; o; o >>= 1) v = fmaxf(v, __shfl_xor(v, o));
  __syncthreads();
  if ((threadIdx.x & 63) == 0) red[threadIdx.x >> 6] = v;
  __syncthreads();
  float s = -INFINITY;
  for (int i = 0; i < (int)(blockDim.x >> 6); ++i) s = fmaxf(s, red[i]);
  return s;
}
__device__ void ln640(const float* xs, float* xn, float* red) {
  float s = 0;
  for (int i = threadIdx.x; i < D; i += blockDim.x) s += xs[i];
  float mean = block_sum(s, red) / D;
  float q = 0;
  for (int i = threadIdx.x; i < D; i += blockDim.x) { float d = xs[i] - mean; q += d * d; }
  float var = rbf(block_sum(q, red) / D);
  float m = rbf(mean);
  float den = rbf(sqrtf(rbf(var + 1e-5f)));
  for (int i = threadIdx.x; i < D; i += blockDim.x) xn[i] = rbf(rbf(xs[i] - m) / den);
  __syncthreads();
}
__global__ void k_q(const u16* x, const u16* wq, float* q) {
  __shared__ float xs[D], xn[D], red[16];
  int b = blockIdx.y;
  for (int i = threadIdx.x; i < D; i += blockDim.x) xs[i] = bf2f(x[b * D + i]);
  __syncthreads();
  ln640(xs, xn, red);
  int n = blockIdx.x * blockDim.x + threadIdx.x;
  float a = 0;
  for (int k = 0; k < D; ++k) a += xn[k] * bf2f(wq[(size_t)k * (H * DH) + n]);
  q[b * H * DH + n] = rbf(a);
}
__global__ void k_attn(const float* q, const u16* kc, const u16* vc, float* o) {
  __shared__ float qs[DH], p[W], red[16];
  int bh = blockIdx.x;
  if (threadIdx.x < DH) qs[threadIdx.x] = q[bh * DH + threadIdx.x];
  __syncthreads();
  const u16* kp = kc + ((size_t)bh * SK + (SK - W) + threadIdx.x) * DH;
  float s = 0;
  for (int d = 0; d < DH; ++d) s += qs[d] * bf2f(kp[d]);
  s = rbf(rbf(s) * 0.08838834764831845f);
  float m = block_max(s, red);
  float e = __expf(s - m);
  float l = block_sum(e, red);
  p[threadIdx.x] = rbf(e / l);
  __syncthreads();
  if (threadIdx.x < DH) {
    const u16* vp = vc + ((size_t)bh * SK + (SK - W)) * DH + threadIdx.x;
    float a = 0;
    for (int k = 0; k < W; ++k) a += p[k] * bf2f(vp[(size_t)k * DH]);
    o[bh * DH + threadIdx.x] = rbf(a);
  }
}
__global__ void k_wo(const float* o, const u16* wo, const u16* x, float* h) {
  __shared__ float part[16][64];
  int b = blockIdx.y, n = blockIdx.x * 64 + (threadIdx.x & 63), w = threadIdx.x >> 6;
  float a = 0;
  for (int k = w * 256; k < w * 256 + 256; ++k) a += o[b * H * DH + k] * bf2f(wo[(size_t)k * D + n]);
  part[w][threadIdx.x & 63] = a;
  __syncthreads();
  if (w == 0) {
    float s = 0;
    for (int i = 0; i < 16; ++i) s += part[i][threadIdx.x];
    h[b * D + n] = rbf(bf2f(x[b * D + n]) + rbf(s));
  }
}
__global__ void k_gu(const float* h, const u16* wg, const u16* wu, float* act) {
  __shared__ float xs[D], xn[D], red[16];
  int b = blockIdx.y;
  for (int i = threadIdx.x; i < D; i += blockDim.x) xs[i] = h[b * D + i];
  __syncthreads();
  ln640(xs, xn, red);
  int n = blockIdx.x * blockDim.x + threadIdx.x;
  float g = 0, u = 0;
  for (int k = 0; k < D; ++k) { g += xn[k] * bf2f(wg[(size_t)k * F + n]); u += xn[k] * bf2f(wu[(size_t)k * F + n]); }
  g = rbf(g); u = rbf(u);
  g = rbf(g / (1.f + __expf(-g)));
  act[b * F + n] = rbf(g * u);
}
__global__ void k_dn(const float* act, const u16* wd, const float* h, __hip_bfloat16* out) {
  __shared__ float part[16][64];
  int b = blockIdx.y, n = blockIdx.x * 64 + (threadIdx.x & 63), w = threadIdx.x >> 6;
  float a = 0;
  for (int k = w * 108; k < w * 108 + 108; ++k) a += act[b * F + k] * bf2f(wd[(size_t)k * D + n]);
  part[w][threadIdx.x & 63] = a;
  __syncthreads();
  if (w == 0) {
    float s = 0;
    for (int i = 0; i < 16; ++i) s += part[i][threadIdx.x];
    out[b * D + n] = __float2bfloat16(h[b * D + n] + rbf(s));
  }
}
extern "C" void kernel_launch(void* const* d_in, const int* in_sizes, int n_in, void* d_out, int out_size, void* d_ws, size_t ws_size, hipStream_t stream) {
  const u16* x = (const u16*)d_in[0]; const u16* kc = (const u16*)d_in[1]; const u16* vc = (const u16*)d_in[2];
  const u16* wq = (const u16*)d_in[3]; const u16* wo = (const u16*)d_in[4];
  const u16* wg = (const u16*)d_in[6]; const u16* wu = (const u16*)d_in[7]; const u16* wd = (const u16*)d_in[8];
  float* ws = (float*)d_ws;
  float* q = ws; float* o = ws + 8192; float* h = ws + 16384; float* act = ws + 20480;
  k_q<<<dim3(64, 2), 64, 0, stream>>>(x, wq, q);
  k_attn<<<64, 1024, 0, stream>>>(q, kc, vc, o);
  k_wo<<<dim3(10, 2), 1024, 0, stream>>>(o, wo, x, h);
  k_gu<<<dim3(27, 2), 64, 0, stream>>>(h, wg, wu, act);
  k_dn<<<dim3(10, 2), 1024, 0, stream>>>(act, wd, h, (__hip_bfloat16*)d_out);
}
```

```cpp
#include <hip/hip_runtime.h>
#include <hip/hip_bf16.h>

#define NT_KV 1
#define NT_W 0
#define WO_FIRST 0
#define FFN_LATE 0
#define STAMP_OFF (64u << 20)
#define NSTAMP 16

typedef unsigned short u16;
typedef unsigned long long u64;
typedef unsigned v4u __attribute__((ext_vector_type(4)));
typedef unsigned v2u __attribute__((ext_vector_type(2)));
typedef float f32x4 __attribute__((ext_vector_type(4)));
typedef float f32x2 __attribute__((ext_vector_type(2)));
typedef __bf16 bf16x2 __attribute__((ext_vector_type(2)));
typedef __bf16 bf16x8 __attribute__((ext_vector_type(8)));
typedef short s16x4 __attribute__((ext_vector_type(4)));
#define GAS __attribute__((address_space(1)))
#define LAS __attribute__((address_space(3)))
#define CAS __attribute__((address_space(4)))

constexpr int D = 640, H = 32, DH = 128, SK = 4096, W = 1024, F = 1728;
constexpr int NWG = 256, NWAVE = 14, NTHR = NWAVE * 64;
constexpr int NFFN = 108;
constexpr int RC = 16, NCG = D / RC, NRED = 2 * NCG;
constexpr unsigned SPIN_G = 200000, SPIN_L = 4000000;

constexpr unsigned WS_EPOCH = 0;
constexpr unsigned WS_QP = 4096;
constexpr unsigned WS_SC = WS_QP + H * 2 * 8 * 128 * 8;
constexpr unsigned WS_T1 = WS_SC + 2 * H * W * 8;
constexpr unsigned WS_HB = WS_T1 + NRED * 128 * RC * 8;
constexpr unsigned WS_T3 = WS_HB + 8192;
constexpr unsigned WS_END = WS_T3 + NRED * 128 * RC * 8;

constexpr int L_FLAG = 0;
constexpr int L_XN = 64;
constexpr int L_Q = L_XN + 2560;
constexpr int L_P = L_Q + 256;
constexpr int L_OP = L_P + 2048;
constexpr int L_ST = L_OP + 2048;
constexpr int L_GU = L_ST + 128;
constexpr int L_ACT = L_GU + 5120;
constexpr int L_RED = L_ACT + 320;
constexpr int L_END = L_RED + 1024;
constexpr int LDS_BYTES = 96 * 1024;

template <int NT> __device__ __forceinline__ v4u ldg(const u16* p) {
  if (NT) return __builtin_nontemporal_load((const GAS v4u*)p);
  return *(const GAS v4u*)p;
}
__device__ __forceinline__ unsigned cvtpk(float lo, float hi) {
  f32x2 v = {lo, hi};
  return __builtin_bit_cast(unsigned, __builtin_convertvector(v, bf16x2));
}
__device__ __forceinline__ float lo2f(unsigned u) { return __uint_as_float(u << 16); }
__device__ __forceinline__ float hi2f(unsigned u) { return __uint_as_float(u & 0xffff0000u); }
__device__ __forceinline__ float rbf(float f) { return lo2f(cvtpk(f, f)); }
__device__ __forceinline__ float dot2(unsigned a, unsigned b, float c) {
  return __builtin_amdgcn_fdot2_f32_bf16(__builtin_bit_cast(bf16x2, a), __builtin_bit_cast(bf16x2, b), c, false);
}
template <int CTRL> __device__ __forceinline__ float dpp(float x) {
  return __builtin_bit_cast(float, __builtin_amdgcn_mov_dpp(__builtin_bit_cast(int, x), CTRL, 0xf, 0xf, true));
}
constexpr int XOR1 = 0xB1, XOR2 = 0x4E, XOR7 = 0x141, XOR8 = 0x128, ROR4 = 0x124;
__device__ __forceinline__ float sum16(float x) { x += dpp<XOR1>(x); x += dpp<XOR2>(x); x += dpp<XOR7>(x); x += dpp<XOR8>(x); return x; }
__device__ __forceinline__ float max16(float x) { x = fmaxf(x, dpp<XOR1>(x)); x = fmaxf(x, dpp<XOR2>(x)); x = fmaxf(x, dpp<XOR7>(x)); x = fmaxf(x, dpp<XOR8>(x)); return x; }
__device__ __forceinline__ float sumx16(float x) {
  auto s = __builtin_amdgcn_permlane16_swap(__float_as_uint(x), __float_as_uint(x), false, false);
  return __uint_as_float(s[0]) + __uint_as_float(s[1]);
}
__device__ __forceinline__ float sumx32(float x) {
  auto s = __builtin_amdgcn_permlane32_swap(__float_as_uint(x), __float_as_uint(x), false, false);
  return __uint_as_float(s[0]) + __uint_as_float(s[1]);
}
__device__ __forceinline__ float maxx16(float x) {
  auto s = __builtin_amdgcn_permlane16_swap(__float_as_uint(x), __float_as_uint(x), false, false);
  return fmaxf(__uint_as_float(s[0]), __uint_as_float(s[1]));
}
__device__ __forceinline__ float maxx32(float x) {
  auto s = __builtin_amdgcn_permlane32_swap(__float_as_uint(x), __float_as_uint(x), false, false);
  return fmaxf(__uint_as_float(s[0]), __uint_as_float(s[1]));
}
__device__ __forceinline__ f32x4 mfma32(v4u a, v4u b, f32x4 c) {
  return __builtin_amdgcn_mfma_f32_16x16x32_bf16(__builtin_bit_cast(bf16x8, a), __builtin_bit_cast(bf16x8, b), c, 0, 0, 0);
}
__device__ __forceinline__ f32x4 mfma16(v2u a, v2u b, f32x4 c) {
  return __builtin_amdgcn_mfma_f32_16x16x16bf16_1k(__builtin_bit_cast(s16x4, a), __builtin_bit_cast(s16x4, b), c, 0, 0, 0);
}
__device__ __forceinline__ void tr8(const v4u* r, v4u* t) {
#pragma unroll
  for (int dw = 0; dw < 4; ++dw)
#pragma unroll
    for (int pr = 0; pr < 4; ++pr) {
      t[2 * dw][pr] = __builtin_amdgcn_perm(r[2 * pr + 1][dw], r[2 * pr][dw], 0x05040100u);
      t[2 * dw + 1][pr] = __builtin_amdgcn_perm(r[2 * pr + 1][dw], r[2 * pr][dw], 0x07060302u);
    }
}
__device__ __forceinline__ void tr4(const v4u* r, v2u* t) {
#pragma unroll
  for (int dw = 0; dw < 4; ++dw)
#pragma unroll
    for (int pr = 0; pr < 2; ++pr) {
      t[2 * dw][pr] = __builtin_amdgcn_perm(r[2 * pr + 1][dw], r[2 * pr][dw], 0x05040100u);
      t[2 * dw + 1][pr] = __builtin_amdgcn_perm(r[2 * pr + 1][dw], r[2 * pr][dw], 0x07060302u);
    }
}
__device__ __forceinline__ float sel4(int g, float a, float b, float c, float d) {
  const float x = (g & 1) ? b : a, y = (g & 1) ? d : c;
  return (g & 2) ? y : x;
}
__device__ __forceinline__ unsigned pack24(float v, unsigned tlo) { return ((__float_as_uint(v) + 0x80u) & 0xffffff00u) | tlo; }
__device__ __forceinline__ float unpack24(unsigned u) { return __uint_as_float(u & 0xffffff00u); }
__device__ __forceinline__ unsigned bad24(v4u g, unsigned tlo, unsigned thi) {
  return (((g.x ^ tlo) | (g.z ^ tlo)) & 0xffu) | (g.y ^ thi) | (g.w ^ thi);
}
constexpr int SC1 = 16;
__device__ __forceinline__ v4u ld_sc1(__amdgpu_buffer_rsrc_t rs, unsigned off) { return __builtin_amdgcn_raw_buffer_load_b128(rs, off, 0, SC1); }
__device__ __forceinline__ void st_sc1(__amdgpu_buffer_rsrc_t rs, unsigned off, v4u v) { __builtin_amdgcn_raw_buffer_store_b128(v, rs, off, 0, SC1); }
__device__ __forceinline__ void st_sc1_64(__amdgpu_buffer_rsrc_t rs, unsigned off, v2u v) { __builtin_amdgcn_raw_buffer_store_b64(v, rs, off, 0, SC1); }
#define MEMBAR() asm volatile("" ::: "memory")
#define LDS_WAIT() asm volatile("s_waitcnt lgkmcnt(0)" ::: "memory")
#define SCHED() __builtin_amdgcn_sched_barrier(0)
#define BAR() do { LDS_WAIT(); __builtin_amdgcn_s_barrier(); MEMBAR(); } while (0)
__device__ __forceinline__ void lds_wait_ge(volatile LAS unsigned* f, unsigned want) {
  for (unsigned n = 0; *f < want && n < SPIN_L; ++n) __builtin_amdgcn_s_sleep(1);
  MEMBAR();
}
__device__ __forceinline__ u64 load_epoch(const unsigned char* ws) {
  u64 e;
  asm volatile("s_load_dwordx2 %0, %1, 0x0\n\ts_waitcnt lgkmcnt(0)" : "=s"(e) : "s"(ws) : "memory");
  return e + 1ull;
}
#define TAGS() const u64 epoch = load_epoch(ws); SCHED(); const u64 tag = epoch * 0x9E3779B97F4A7C15ull; \
  const unsigned thi = (unsigned)tag, tlo = (unsigned)(tag >> 32) & 0xffu, t16 = (unsigned)(tag >> 32) & 0xffffu; (void)tlo; (void)t16
#define PIN4(a) asm volatile("" : "+v"((a)[0]), "+v"((a)[1]), "+v"((a)[2]), "+v"((a)[3]))
#define PIN8(a) do { PIN4(a); PIN4((a) + 4); } while (0)
#define STAMP(k) do {} while (0)

__device__ __forceinline__ float reduce_inbox(__amdgpu_buffer_rsrc_t rs, unsigned base, int nprod, int rw, int lane, unsigned tlo, unsigned thi, LAS float* red) {
  const int p0 = rw * 16 + (lane >> 3), p1 = p0 + 8;
  const unsigned off = base + (unsigned)rw * 2048u + (unsigned)lane * 16u;
  v4u a, b;
  _Pragma("nounroll") for (unsigned n = 0; n < SPIN_G; ++n) {
    a = ld_sc1(rs, off); b = ld_sc1(rs, off + 1024u);
    const unsigned bad = (p0 < nprod ? bad24(a, tlo, thi) : 0u) | (p1 < nprod ? bad24(b, tlo, thi) : 0u);
    if (__all(bad == 0u)) break;
    MEMBAR();
  }
  float s0 = (p0 < nprod ? unpack24(a.x) : 0.f) + (p1 < nprod ? unpack24(b.x) : 0.f);
  float s1 = (p0 < nprod ? unpack24(a.z) : 0.f) + (p1 < nprod ? unpack24(b.z) : 0.f);
  s0 += dpp<XOR8>(s0); s1 += dpp<XOR8>(s1);
  s0 = sumx32(sumx16(s0)); s1 = sumx32(sumx16(s1));
  if (lane < 8) *(LAS f32x2*)(red + rw * 16 + lane * 2) = f32x2{s0, s1};
  BAR();
  const int g = lane >> 4, c = lane & 15;
  float t = red[(2 * g) * 16 + c] + red[(2 * g + 1) * 16 + c];
  return sumx32(sumx16(t));
}

__global__ void __launch_bounds__(NTHR, 4) fused_block(const u16* __restrict__ x, const u16* __restrict__ kc, const u16* __restrict__ vc, const u16* __restrict__ wq,
                                                       const u16* __restrict__ wo, const u16* __restrict__ wg, const u16* __restrict__ wu, const u16* __restrict__ wd,
                                                       u16* __restrict__ out, unsigned char* ws) {
  extern __shared__ __attribute__((aligned(16))) unsigned char lds_raw[];
  LAS unsigned char* L = (LAS unsigned char*)lds_raw;
  volatile LAS unsigned* flag = (volatile LAS unsigned*)(L + L_FLAG);
  const int tid = threadIdx.x, lane = tid & 63, wv = __builtin_amdgcn_readfirstlane(tid >> 6), w = blockIdx.x;
  const int g = lane >> 4, cs = lane & 15;
  const int g8 = w & 7, idx = w >> 3, h = g8 + 8 * (idx & 3), j = idx >> 2, b = j & 1, s = j >> 1;
  const int lg = g8 + 8 * (idx >> 2), f = 4 * lg + (idx & 3);
  const bool is_ffn = idx < 16 && lg < 27;
  const int r = (idx - 16) * 8 + g8;
  const bool is_red = idx >= 16 && r < NRED;
  const __amdgpu_buffer_rsrc_t rs = __builtin_amdgcn_make_buffer_rsrc(ws, 0, WS_END, 0x00020000);
  if (tid < 16) flag[tid] = 0u;
  asm volatile("" :: "s"(x), "s"(kc), "s"(vc), "s"(wq), "s"(wo), "s"(wg), "s"(wu), "s"(wd), "s"(out), "s"(ws));
  u64 epoch_r = 0; unsigned thi_r = 0, tlo_r = 0;

  if (wv == 0) {
    STAMP(0);
    __builtin_amdgcn_s_setprio(3);
    v2u xr[5];
#pragma unroll
    for (int t = 0; t < 5; ++t) xr[t] = *(const GAS v2u*)(x + (lane >> 5) * D + t * 128 + (lane & 31) * 4);
    v4u rq[20];
    const __amdgpu_buffer_rsrc_t rq_rs = __builtin_amdgcn_make_buffer_rsrc((void*)(wq + (size_t)g8 * 80 * (H * DH) + idx * DH), 0, (79 * H * DH + DH) * 2, 0x00020000);
#pragma unroll
    for (int i = 0; i < 20; ++i) rq[i] = __builtin_amdgcn_raw_buffer_load_b128(rq_rs, (unsigned)(4 * g * (H * DH) + cs * 8) * 2u, ((i >> 2) * 16 + (i & 3)) * (H * DH) * 2, NT_W ? 2 : 0);
    SCHED();
    BAR();
    SCHED();
    TAGS();
    float xv[20];
#pragma unroll
    for (int t = 0; t < 5; ++t) { xv[4 * t] = lo2f(xr[t].x); xv[4 * t + 1] = hi2f(xr[t].x); xv[4 * t + 2] = lo2f(xr[t].y); xv[4 * t + 3] = hi2f(xr[t].y); }
    float sm = 0.f;
#pragma unroll
    for (int i = 0; i < 20; ++i) sm += xv[i];
    const float mean = sumx16(sum16(sm)) * (1.f / D);
    float sq = 0.f;
#pragma unroll
    for (int i = 0; i < 20; ++i) { const float d = xv[i] - mean; sq = fmaf(d, d, sq); }
    const float var = rbf(sumx16(sum16(sq)) * (1.f / D)), mb = rbf(mean);
    const float rden = __builtin_amdgcn_rcpf(rbf(__builtin_amdgcn_sqrtf(rbf(var + 1e-5f))));
#pragma unroll
    for (int t = 0; t < 5; ++t) {
      const unsigned d0 = cvtpk(xv[4 * t] - mb, xv[4 * t + 1] - mb), d1 = cvtpk(xv[4 * t + 2] - mb, xv[4 * t + 3] - mb);
      *(LAS v2u*)(L + L_XN + ((lane >> 5) * D + t * 128 + (lane & 31) * 4) * 2) = v2u{cvtpk(lo2f(d0) * rden, hi2f(d0) * rden), cvtpk(lo2f(d1) * rden, hi2f(d1) * rden)};
    }
    LDS_WAIT();
    v2u af[5];
#pragma unroll
    for (int t = 0; t < 5; ++t) af[t] = *(const LAS v2u*)(L + L_XN + ((lane & 1) * D + g8 * 80 + t * 16 + 4 * g) * 2);
    f32x4 acc[8];
#pragma unroll
    for (int c = 0; c < 8; ++c) acc[c] = f32x4{0.f, 0.f, 0.f, 0.f};
#pragma unroll
    for (int t = 0; t < 5; ++t) {
      v2u bt[8];
      tr4(rq + 4 * t, bt);
#pragma unroll
      for (int c = 0; c < 8; ++c) acc[c] = mfma16(af[t], bt[c], acc[c]);
    }
#pragma unroll
    for (int bb = 0; bb < 2; ++bb) {
      const float v0 = sel4(g, acc[0][bb], acc[2][bb], acc[4][bb], acc[6][bb]), v1 = sel4(g, acc[1][bb], acc[3][bb], acc[5][bb], acc[7][bb]);
      st_sc1(rs, WS_QP + (unsigned)(((idx * 2 + bb) * 8 + g8) * 128 + cs * 8 + 2 * g) * 8u, v4u{pack24(v0, tlo), thi, pack24(v1, tlo), thi});
    }
    STAMP(1);
    {
      v4u qg[8];
      const unsigned off = WS_QP + (unsigned)((h * 2 + b) * 8 * 128) * 8u + (unsigned)lane * 16u;
      _Pragma("nounroll") for (unsigned n = 0; n < SPIN_G; ++n) {
        unsigned bad = 0u;
#pragma unroll
        for (int i = 0; i < 8; ++i) qg[i] = ld_sc1(rs, off + i * 1024u);
#pragma unroll
        for (int i = 0; i < 8; ++i) bad |= bad24(qg[i], tlo, thi);
        if (__all(bad == 0u)) break;
        MEMBAR();
      }
      float q0 = 0.f, q1 = 0.f;
#pragma unroll
      for (int i = 0; i < 8; ++i) { q0 += unpack24(qg[i].x); q1 += unpack24(qg[i].z); }
      *(LAS unsigned*)(L + L_Q + lane * 4) = cvtpk(q0, q1);
      LDS_WAIT();
      flag[0] = 1u;
    }
    STAMP(2);
    lds_wait_ge(flag + 1, 4u);
    {
      v4u sg[8];
      const unsigned off = WS_SC + (unsigned)((b * H + h) * W) * 8u + (unsigned)lane * 16u;
      _Pragma("nounroll") for (unsigned n = 0; n < SPIN_G; ++n) {
        unsigned bad = 0u;
#pragma unroll
        for (int i = 0; i < 8; ++i) sg[i] = ld_sc1(rs, off + i * 1024u);
#pragma unroll
        for (int i = 0; i < 8; ++i) bad |= (((sg[i].x ^ t16) | (sg[i].z ^ t16)) & 0xffffu) | (sg[i].y ^ thi) | (sg[i].w ^ thi);
        if (__all(bad == 0u)) break;
        MEMBAR();
      }
      STAMP(4);
      float sv[16], m = -INFINITY;
#pragma unroll
      for (int i = 0; i < 8; ++i) { sv[2 * i] = hi2f(sg[i].x); sv[2 * i + 1] = hi2f(sg[i].z); m = fmaxf(m, fmaxf(sv[2 * i], sv[2 * i + 1])); }
      m = maxx32(maxx16(max16(m)));
      float l = 0.f;
#pragma unroll
      for (int i = 0; i < 16; ++i) { sv[i] = __builtin_amdgcn_exp2f((sv[i] - m) * 1.4426950408889634f); l += sv[i]; }
      const float rl = __builtin_amdgcn_rcpf(sumx32(sumx16(sum16(l))));
#pragma unroll
      for (int i = 0; i < 8; ++i) *(LAS unsigned*)(L + L_P + (i * 128 + lane * 2) * 2) = cvtpk(sv[2 * i] * rl, sv[2 * i + 1] * rl);
      LDS_WAIT();
      flag[2] = 1u;
    }
    STAMP(5);
    return;
  }

  if (wv <= 4) {
    __builtin_amdgcn_s_setprio(2);
    const int kw = wv - 1;
    const u16* kb = kc + ((size_t)(b * H + h) * SK + (SK - W) + s * 256 + kw * 64 + g) * DH + cs * 8;
    v4u rk[16];
#pragma unroll
    for (int i = 0; i < 4; ++i) rk[i] = ldg<NT_KV>(kb + (size_t)i * 4 * DH);
    SCHED();
    BAR();
#pragma unroll
    for (int i = 4; i < 16; ++i) rk[i] = ldg<NT_KV>(kb + (size_t)i * 4 * DH);
    SCHED();
    TAGS();
    epoch_r = epoch; thi_r = thi; tlo_r = tlo;
    lds_wait_ge(flag + 0, 1u);
    const v4u qf = *(const LAS v4u*)(L + L_Q + cs * 16);
    float xs[16];
#pragma unroll
    for (int i = 0; i < 16; ++i) xs[i] = dot2(rk[i].w, qf.w, dot2(rk[i].z, qf.z, dot2(rk[i].y, qf.y, dot2(rk[i].x, qf.x, 0.f))));
#define TR_STEP(HALF, CTRL, BIT) _Pragma("unroll") for (int i = 0; i < (HALF); ++i) { \
      const float t1 = xs[i] + dpp<CTRL>(xs[i]), t2 = xs[i + (HALF)] + dpp<CTRL>(xs[i + (HALF)]); xs[i] = (cs & (BIT)) ? t2 : t1; }
    TR_STEP(8, XOR8, 8) TR_STEP(4, XOR7, 4) TR_STEP(2, XOR2, 2) TR_STEP(1, XOR1, 1)
#undef TR_STEP
    const float sc = rbf(xs[0]) * 0.08838834764831845f;
    st_sc1_64(rs, WS_SC + (unsigned)((b * H + h) * W + s * 256 + kw * 64 + 4 * cs + g) * 8u, v2u{(cvtpk(sc, sc) & 0xffff0000u) | t16, thi});
    if (lane == 0) __hip_atomic_fetch_add((LAS unsigned*)(L + L_FLAG) + 1, 1u, __ATOMIC_RELAXED, __HIP_MEMORY_SCOPE_WORKGROUP);
    if (wv == 1) STAMP(3);
    if (!is_red) return;
    BAR();
  } else if (wv <= 8) {
    __builtin_amdgcn_s_setprio(1);
    BAR();
    const int vw = wv - 5;
    const u16* vb = vc + ((size_t)(b * H + h) * SK + (SK - W) + vw * 256 + (cs >> 2) * 32 + g * 8) * DH + s * 32 + (cs & 3) * 8;
    v4u rv[16];
#pragma unroll
    for (int i = 0; i < 16; ++i) rv[i] = ldg<NT_KV>(vb + (size_t)((i >> 3) * 128 + (i & 7)) * DH);
    SCHED();
    v4u bt[16];
    TAGS();
    epoch_r = epoch; thi_r = thi; tlo_r = tlo;
    tr8(rv, bt); tr8(rv + 8, bt + 8);
    PIN8(bt); PIN8(bt + 8);
    if (wv == 5) STAMP(6);
    lds_wait_ge(flag + 2, 1u);
    const v4u p0 = *(const LAS v4u*)(L + L_P + (vw * 256 + (cs >> 2) * 32 + g * 8) * 2);
    const v4u p1 = *(const LAS v4u*)(L + L_P + (vw * 256 + 128 + (cs >> 2) * 32 + g * 8) * 2);
    f32x4 acc[8];
#pragma unroll
    for (int c = 0; c < 8; ++c) acc[c] = mfma32(p1, bt[8 + c], mfma32(p0, bt[c], f32x4{0.f, 0.f, 0.f, 0.f}));
    if ((cs >> 2) == g) {
      LAS f32x4* o = (LAS f32x4*)(L + L_OP + ((vw * 4 + g) * 32 + (cs & 3) * 8) * 4);
      o[0] = f32x4{acc[0][0], acc[1][0], acc[2][0], acc[3][0]};
      o[1] = f32x4{acc[4][0], acc[5][0], acc[6][0], acc[7][0]};
    }
    BAR();
    if (!is_red) return;
  } else {
    const int ww = wv - 9;
    BAR();
    v4u ro[8], rg[8], rd[4];
    const u16* ob = wo + (size_t)(h * DH + s * 32 + g * 8) * D + ww * 128 + cs * 8;
#pragma unroll
    for (int i = 0; i < 8; ++i) ro[i] = ldg<NT_W>(ob + (size_t)i * D);
    SCHED();
    const u16* gb = ((cs & 2) ? wu : wg) + (size_t)(ww * 128 + (cs >> 2) * 32 + g * 8) * F + f * 16 + (cs & 1) * 8;
    const u16* db = wd + (size_t)(f * 16 + 4 * g) * D + ww * 128 + cs * 8;
    TAGS();
    v4u bo[8];
    tr8(ro, bo);
    PIN8(bo);
    if (is_ffn) {
#pragma unroll
      for (int i = 0; i < 8; ++i) rg[i] = ldg<NT_W>(gb + (size_t)i * F);
#pragma unroll
      for (int i = 0; i < 4; ++i) rd[i] = ldg<NT_W>(db + (size_t)i * D);
    }
    SCHED();
    if (wv == 9) STAMP(7);
    BAR();
    if (wv == 9) STAMP(8);
    {
      const LAS f32x4* op = (const LAS f32x4*)(L + L_OP + (cs * 32 + g * 8) * 4);
      const f32x4 a0 = op[0], a1 = op[1];
      const v4u af = {cvtpk(sum16(a0[0]), sum16(a0[1])), cvtpk(sum16(a0[2]), sum16(a0[3])), cvtpk(sum16(a1[0]), sum16(a1[1])), cvtpk(sum16(a1[2]), sum16(a1[3]))};
      f32x4 acc[8];
#pragma unroll
      for (int c = 0; c < 8; ++c) acc[c] = mfma32(af, bo[c], f32x4{0.f, 0.f, 0.f, 0.f});
      const float v0 = sel4(g, acc[0][0], acc[2][0], acc[4][0], acc[6][0]), v1 = sel4(g, acc[1][0], acc[3][0], acc[5][0], acc[7][0]);
      const int col = ww * 128 + cs * 8 + 2 * g;
      st_sc1(rs, WS_T1 + (unsigned)(((b * NCG + col / RC) * 128 + h * 4 + s) * RC + col % RC) * 8u, v4u{pack24(v0, tlo), thi, pack24(v1, tlo), thi});
    }
    if (wv == 9) STAMP(9);
    if (!is_ffn) return;
    v4u bg[8]; v2u bd[8];
    tr8(rg, bg); tr4(rd, bd);
    PIN8(bg); PIN8(bd);
    v4u hg;
    {
      const unsigned off = WS_HB + (unsigned)((lane >> 5) * 320 + ww * 64 + (lane & 31) * 2) * 8u;
      _Pragma("nounroll") for (unsigned n = 0; n < SPIN_G; ++n) {
        hg = ld_sc1(rs, off);
        if (__all(((hg.y ^ thi) | (hg.w ^ thi)) == 0u)) break;
        MEMBAR();
      }
    }
    if (wv == 9) STAMP(11);
    const float h0 = lo2f(hg.x), h1 = hi2f(hg.x), h2 = lo2f(hg.z), h3 = hi2f(hg.z);
    const float ls = sumx16(sum16((h0 + h1) + (h2 + h3)));
    const float lm = ls * (1.f / 128.f);
    const float e0 = h0 - lm, e1 = h1 - lm, e2 = h2 - lm, e3 = h3 - lm;
    const float lq = sumx16(sum16(fmaf(e0, e0, e1 * e1) + fmaf(e2, e2, e3 * e3)));
    if ((lane & 31) == 0) *(LAS f32x2*)(L + L_ST + (ww * 2 + (lane >> 5)) * 8) = f32x2{ls, lq};
    BAR();
    f32x2 st[5];
#pragma unroll
    for (int i = 0; i < 5; ++i) st[i] = *(const LAS f32x2*)(L + L_ST + (i * 2 + (lane >> 5)) * 8);
    const float mean = (((st[0][0] + st[1][0]) + (st[2][0] + st[3][0])) + st[4][0]) * (1.f / D);
    float m2 = 0.f;
#pragma unroll
    for (int i = 0; i < 5; ++i) { const float d = st[i][0] * (1.f / 128.f) - mean; m2 += fmaf(128.f * d, d, st[i][1]); }
    const float var = rbf(m2 * (1.f / D)), mb = rbf(mean);
    const float rden = __builtin_amdgcn_rcpf(rbf(__builtin_amdgcn_sqrtf(rbf(var + 1e-5f))));
    const unsigned d0 = cvtpk(h0 - mb, h1 - mb), d1 = cvtpk(h2 - mb, h3 - mb);
    *(LAS v2u*)(L + L_XN + ((lane >> 5) * D + ww * 128 + (lane & 31) * 4) * 2) = v2u{cvtpk(lo2f(d0) * rden, hi2f(d0) * rden), cvtpk(lo2f(d1) * rden, hi2f(d1) * rden)};
    LDS_WAIT();
    {
      const v4u af = *(const LAS v4u*)(L + L_XN + ((lane & 1) * D + ww * 128 + (cs >> 2) * 32 + g * 8) * 2);
      f32x4 acc[8];
#pragma unroll
      for (int c = 0; c < 8; ++c) acc[c] = mfma32(af, bg[c], f32x4{0.f, 0.f, 0.f, 0.f});
      if ((cs >> 2) == g) {
#pragma unroll
        for (int bb = 0; bb < 2; ++bb) {
          LAS f32x4* o = (LAS f32x4*)(L + L_GU + ((ww * 4 + g) * 64 + ((cs >> 1) & 1) * 32 + bb * 16 + (cs & 1) * 8) * 4);
          o[0] = f32x4{acc[0][bb], acc[1][bb], acc[2][bb], acc[3][bb]};
          o[1] = f32x4{acc[4][bb], acc[5][bb], acc[6][bb], acc[7][bb]};
        }
      }
    }
    BAR();
    {
      const LAS float* gu = (const LAS float*)(L + L_GU) + lane;
      float t[20];
#pragma unroll
      for (int i = 0; i < 20; ++i) t[i] = gu[i * 64];
      float a = 0.f;
#pragma unroll
      for (int i = 0; i < 5; ++i) a += (t[4 * i] + t[4 * i + 1]) + (t[4 * i + 2] + t[4 * i + 3]);
      a = rbf(a);
      auto sw = __builtin_amdgcn_permlane32_swap(__float_as_uint(a), __float_as_uint(a), false, false);
      const float gv = __uint_as_float(sw[0]), uv = __uint_as_float(sw[1]);
      const float sg = rbf(gv * __builtin_amdgcn_rcpf(1.f + __builtin_amdgcn_exp2f(gv * -1.4426950408889634f)));
      const unsigned av = cvtpk(sg * uv, 0.f);
      if (lane < 32) *(LAS u16*)(L + L_ACT + ww * 64 + lane * 2) = (u16)av;
      LDS_WAIT();
      const v2u af = *(const LAS v2u*)(L + L_ACT + ww * 64 + (lane & 1) * 32 + g * 8);
      f32x4 acc[8];
#pragma unroll
      for (int c = 0; c < 8; ++c) acc[c] = mfma16(af, bd[c], f32x4{0.f, 0.f, 0.f, 0.f});
      const int col = ww * 128 + cs * 8 + 2 * g;
#pragma unroll
      for (int bb = 0; bb < 2; ++bb) {
        const float v0 = sel4(g, acc[0][bb], acc[2][bb], acc[4][bb], acc[6][bb]), v1 = sel4(g, acc[1][bb], acc[3][bb], acc[5][bb], acc[7][bb]);
        st_sc1(rs, WS_T3 + (unsigned)(((bb * NCG + col / RC) * 128 + f) * RC + col % RC) * 8u, v4u{pack24(v0, tlo), thi, pack24(v1, tlo), thi});
      }
    }
    if (wv == 9) STAMP(12);
    return;
  }

  const int rw = wv - 1, rb = r / NCG, rc0 = (r % NCG) * RC;
  const u64 epoch = epoch_r; const unsigned thi = thi_r, tlo = tlo_r;
  const float xv = lo2f(x[rb * D + rc0 + cs]);
  const float a1 = reduce_inbox(rs, WS_T1 + (unsigned)r * (128u * RC * 8u), 128, rw, lane, tlo, thi, (LAS float*)(L + L_RED));
  const float hv = rbf(xv + rbf(a1));
  if (rw == 0) {
    const float hn = dpp<XOR1>(hv);
    if (lane < 16 && !(lane & 1)) st_sc1_64(rs, WS_HB + (unsigned)(rb * 320 + (rc0 + lane) / 2) * 8u, v2u{cvtpk(hv, hn), thi});
    STAMP(10);
  }
  const float a2 = reduce_inbox(rs, WS_T3 + (unsigned)r * (128u * RC * 8u), NFFN, rw, lane, tlo, thi, (LAS float*)(L + L_RED + 512));
  if (rw == 0) {
    const float ov = hv + rbf(a2);
    const float on = dpp<XOR1>(ov);
    if (lane < 16 && !(lane & 1)) *(GAS unsigned*)(out + rb * D + rc0 + lane) = cvtpk(ov, on);
    if (r == 0 && lane == 0) *(GAS u64*)(ws + WS_EPOCH) = epoch;
    STAMP(13);
  }
}

extern "C" void kernel_launch(void* const* d_in, const int* in_sizes, int n_in, void* d_out, int out_size, void* d_ws, size_t ws_size, hipStream_t stream) {
  (void)hipFuncSetAttribute((const void*)fused_block, hipFuncAttributeMaxDynamicSharedMemorySize, LDS_BYTES);
  fused_block<<<NWG, NTHR, LDS_BYTES, stream>>>((const u16*)d_in[0], (const u16*)d_in[1], (const u16*)d_in[2], (const u16*)d_in[3], (const u16*)d_in[4],
                                               (const u16*)d_in[6], (const u16*)d_in[7], (const u16*)d_in[8], (u16*)d_out, (unsigned char*)d_ws);
}
```

```cpp
#define PADT 1800ull
#include <hip/hip_runtime.h>
#include <hip/hip_bf16.h>

#define NT_KV 1
#define NT_W 1
#define WO_FIRST 0
#define FFN_LATE 0
#define STAMP_OFF (64u << 20)
#define NSTAMP 16

typedef unsigned short u16;
typedef unsigned long long u64;
typedef unsigned v4u __attribute__((ext_vector_type(4)));
typedef unsigned v2u __attribute__((ext_vector_type(2)));
typedef float f32x4 __attribute__((ext_vector_type(4)));
typedef float f32x2 __attribute__((ext_vector_type(2)));
typedef __bf16 bf16x2 __attribute__((ext_vector_type(2)));
typedef __bf16 bf16x8 __attribute__((ext_vector_type(8)));
typedef short s16x4 __attribute__((ext_vector_type(4)));
#define GAS __attribute__((address_space(1)))
#define LAS __attribute__((address_space(3)))
#define CAS __attribute__((address_space(4)))

constexpr int D = 640, H = 32, DH = 128, SK = 4096, W = 1024, F = 1728;
constexpr int NWG = 256, NWAVE = 14, NTHR = NWAVE * 64;
constexpr int NFFN = 108;
constexpr int RC = 16, NCG = D / RC, NRED = 2 * NCG;
constexpr unsigned SPIN_G = 200000, SPIN_L = 4000000;

constexpr unsigned WS_EPOCH = 0;
constexpr unsigned WS_QP = 4096;
constexpr unsigned WS_SC = WS_QP + H * 2 * 8 * 128 * 8;
constexpr unsigned WS_T1 = WS_SC + 2 * H * W * 8;
constexpr unsigned WS_HB = WS_T1 + NRED * 128 * RC * 8;
constexpr unsigned WS_T3 = WS_HB + 8192;
constexpr unsigned WS_END = WS_T3 + NRED * 128 * RC * 8;

constexpr int L_FLAG = 0;
constexpr int L_XN = 64;
constexpr int L_Q = L_XN + 2560;
constexpr int L_P = L_Q + 256;
constexpr int L_OP = L_P + 2048;
constexpr int L_ST = L_OP + 2048;
constexpr int L_GU = L_ST + 128;
constexpr int L_ACT = L_GU + 5120;
constexpr int L_RED = L_ACT + 320;
constexpr int L_END = L_RED + 1024;
constexpr int LDS_BYTES = 96 * 1024;

template <int NT> __device__ __forceinline__ v4u ldg(const u16* p) {
  if (NT) return __builtin_nontemporal_load((const GAS v4u*)p);
  return *(const GAS v4u*)p;
}
__device__ __forceinline__ unsigned cvtpk(float lo, float hi) {
  f32x2 v = {lo, hi};
  return __builtin_bit_cast(unsigned, __builtin_convertvector(v, bf16x2));
}
__device__ __forceinline__ float lo2f(unsigned u) { return __uint_as_float(u << 16); }
__device__ __forceinline__ float hi2f(unsigned u) { return __uint_as_float(u & 0xffff0000u); }
__device__ __forceinline__ float rbf(float f) { return lo2f(cvtpk(f, f)); }
__device__ __forceinline__ float dot2(unsigned a, unsigned b, float c) {
  return __builtin_amdgcn_fdot2_f32_bf16(__builtin_bit_cast(bf16x2, a), __builtin_bit_cast(bf16x2, b), c, false);
}
template <int CTRL> __device__ __forceinline__ float dpp(float x) {
  return __builtin_bit_cast(float, __builtin_amdgcn_mov_dpp(__builtin_bit_cast(int, x), CTRL, 0xf, 0xf, true));
}
constexpr int XOR1 = 0xB1, XOR2 = 0x4E, XOR7 = 0x141, XOR8 = 0x128, ROR4 = 0x124;
__device__ __forceinline__ float sum16(float x) { x += dpp<XOR1>(x); x += dpp<XOR2>(x); x += dpp<XOR7>(x); x += dpp<XOR8>(x); return x; }
__device__ __forceinline__ float max16(float x) { x = fmaxf(x, dpp<XOR1>(x)); x = fmaxf(x, dpp<XOR2>(x)); x = fmaxf(x, dpp<XOR7>(x)); x = fmaxf(x, dpp<XOR8>(x)); return x; }
__device__ __forceinline__ float sumx16(float x) {
  auto s = __builtin_amdgcn_permlane16_swap(__float_as_uint(x), __float_as_uint(x), false, false);
  return __uint_as_float(s[0]) + __uint_as_float(s[1]);
}
__device__ __forceinline__ float sumx32(float x) {
  auto s = __builtin_amdgcn_permlane32_swap(__float_as_uint(x), __float_as_uint(x), false, false);
  return __uint_as_float(s[0]) + __uint_as_float(s[1]);
}
__device__ __forceinline__ float maxx16(float x) {
  auto s = __builtin_amdgcn_permlane16_swap(__float_as_uint(x), __float_as_uint(x), false, false);
  return fmaxf(__uint_as_float(s[0]), __uint_as_float(s[1]));
}
__device__ __forceinline__ float maxx32(float x) {
  auto s = __builtin_amdgcn_permlane32_swap(__float_as_uint(x), __float_as_uint(x), false, false);
  return fmaxf(__uint_as_float(s[0]), __uint_as_float(s[1]));
}
__device__ __forceinline__ f32x4 mfma32(v4u a, v4u b, f32x4 c) {
  return __builtin_amdgcn_mfma_f32_16x16x32_bf16(__builtin_bit_cast(bf16x8, a), __builtin_bit_cast(bf16x8, b), c, 0, 0, 0);
}
__device__ __forceinline__ f32x4 mfma16(v2u a, v2u b, f32x4 c) {
  return __builtin_amdgcn_mfma_f32_16x16x16bf16_1k(__builtin_bit_cast(s16x4, a), __builtin_bit_cast(s16x4, b), c, 0, 0, 0);
}
__device__ __forceinline__ void tr8(const v4u* r, v4u* t) {
#pragma unroll
  for (int dw = 0; dw < 4; ++dw)
#pragma unroll
    for (int pr = 0; pr < 4; ++pr) {
      t[2 * dw][pr] = __builtin_amdgcn_perm(r[2 * pr + 1][dw], r[2 * pr][dw], 0x05040100u);
      t[2 * dw + 1][pr] = __builtin_amdgcn_perm(r[2 * pr + 1][dw], r[2 * pr][dw], 0x07060302u);
    }
}
__device__ __forceinline__ void tr4(const v4u* r, v2u* t) {
#pragma unroll
  for (int dw = 0; dw < 4; ++dw)
#pragma unroll
    for (int pr = 0; pr < 2; ++pr) {
      t[2 * dw][pr] = __builtin_amdgcn_perm(r[2 * pr + 1][dw], r[2 * pr][dw], 0x05040100u);
      t[2 * dw + 1][pr] = __builtin_amdgcn_perm(r[2 * pr + 1][dw], r[2 * pr][dw], 0x07060302u);
    }
}
__device__ __forceinline__ float sel4(int g, float a, float b, float c, float d) {
  const float x = (g & 1) ? b : a, y = (g & 1) ? d : c;
  return (g & 2) ? y : x;
}
__device__ __forceinline__ unsigned pack24(float v, unsigned tlo) { return ((__float_as_uint(v) + 0x80u) & 0xffffff00u) | tlo; }
__device__ __forceinline__ float unpack24(unsigned u) { return __uint_as_float(u & 0xffffff00u); }
__device__ __forceinline__ unsigned bad24(v4u g, unsigned tlo, unsigned thi) {
  return (((g.x ^ tlo) | (g.z ^ tlo)) & 0xffu) | (g.y ^ thi) | (g.w ^ thi);
}
constexpr int SC1 = 16;
__device__ __forceinline__ v4u ld_sc1(__amdgpu_buffer_rsrc_t rs, unsigned off) { return __builtin_amdgcn_raw_buffer_load_b128(rs, off, 0, SC1); }
__device__ __forceinline__ void st_sc1(__amdgpu_buffer_rsrc_t rs, unsigned off, v4u v) { __builtin_amdgcn_raw_buffer_store_b128(v, rs, off, 0, SC1); }
__device__ __forceinline__ void st_sc1_64(__amdgpu_buffer_rsrc_t rs, unsigned off, v2u v) { __builtin_amdgcn_raw_buffer_store_b64(v, rs, off, 0, SC1); }
#define MEMBAR() asm volatile("" ::: "memory")
#define LDS_WAIT() asm volatile("s_waitcnt lgkmcnt(0)" ::: "memory")
#define SCHED() __builtin_amdgcn_sched_barrier(0)
#define BAR() do { LDS_WAIT(); __builtin_amdgcn_s_barrier(); MEMBAR(); } while (0)
__device__ __forceinline__ void lds_wait_ge(volatile LAS unsigned* f, unsigned want) {
  for (unsigned n = 0; *f < want && n < SPIN_L; ++n) __builtin_amdgcn_s_sleep(1);
  MEMBAR();
}
__device__ __forceinline__ u64 load_epoch(const unsigned char* ws) {
  u64 e;
  asm volatile("s_load_dwordx2 %0, %1, 0x0\n\ts_waitcnt lgkmcnt(0)" : "=s"(e) : "s"(ws) : "memory");
  return e + 1ull;
}
#define TAGS() const u64 epoch = load_epoch(ws); SCHED(); const u64 tag = epoch * 0x9E3779B97F4A7C15ull; \
  const unsigned thi = (unsigned)tag, tlo = (unsigned)(tag >> 32) & 0xffu, t16 = (unsigned)(tag >> 32) & 0xffffu; (void)tlo; (void)t16
#define PIN4(a) asm volatile("" : "+v"((a)[0]), "+v"((a)[1]), "+v"((a)[2]), "+v"((a)[3]))
#define PIN8(a) do { PIN4(a); PIN4((a) + 4); } while (0)
#define STAMP(k) do {} while (0)

__device__ __forceinline__ float reduce_inbox(__amdgpu_buffer_rsrc_t rs, unsigned base, int nprod, int rw, int lane, unsigned tlo, unsigned thi, LAS float* red) {
  const int p0 = rw * 16 + (lane >> 3), p1 = p0 + 8;
  const unsigned off = base + (unsigned)rw * 2048u + (unsigned)lane * 16u;
  v4u a, b;
  _Pragma("nounroll") for (unsigned n = 0; n < SPIN_G; ++n) {
    a = ld_sc1(rs, off); b = ld_sc1(rs, off + 1024u);
    const unsigned bad = (p0 < nprod ? bad24(a, tlo, thi) : 0u) | (p1 < nprod ? bad24(b, tlo, thi) : 0u);
    if (__all(bad == 0u)) break;
    MEMBAR();
  }
  float s0 = (p0 < nprod ? unpack24(a.x) : 0.f) + (p1 < nprod ? unpack24(b.x) : 0.f);
  float s1 = (p0 < nprod ? unpack24(a.z) : 0.f) + (p1 < nprod ? unpack24(b.z) : 0.f);
  s0 += dpp<XOR8>(s0); s1 += dpp<XOR8>(s1);
  s0 = sumx32(sumx16(s0)); s1 = sumx32(sumx16(s1));
  if (lane < 8) *(LAS f32x2*)(red + rw * 16 + lane * 2) = f32x2{s0, s1};
  BAR();
  const int g = lane >> 4, c = lane & 15;
  float t = red[(2 * g) * 16 + c] + red[(2 * g + 1) * 16 + c];
  return sumx32(sumx16(t));
}

__global__ void __launch_bounds__(NTHR, 4) fused_block(const u16* __restrict__ x, const u16* __restrict__ kc, const u16* __restrict__ vc, const u16* __restrict__ wq,
                                                       const u16* __restrict__ wo, const u16* __restrict__ wg, const u16* __restrict__ wu, const u16* __restrict__ wd,
                                                       u16* __restrict__ out, unsigned char* ws) {
  extern __shared__ __attribute__((aligned(16))) unsigned char lds_raw[];
  LAS unsigned char* L = (LAS unsigned char*)lds_raw;
  volatile LAS unsigned* flag = (volatile LAS unsigned*)(L + L_FLAG);
  const int tid = threadIdx.x, lane = tid & 63, wv = __builtin_amdgcn_readfirstlane(tid >> 6), w = blockIdx.x;
  const int g = lane >> 4, cs = lane & 15;
  const int g8 = w & 7, idx = w >> 3, h = g8 + 8 * (idx & 3), j = idx >> 2, b = j & 1, s = j >> 1;
  const int lg = g8 + 8 * (idx >> 2), f = 4 * lg + (idx & 3);
  const bool is_ffn = idx < 16 && lg < 27;
  const int r = (idx - 16) * 8 + g8;
  const bool is_red = idx >= 16 && r < NRED;
  const __amdgpu_buffer_rsrc_t rs = __builtin_amdgcn_make_buffer_rsrc(ws, 0, WS_END, 0x00020000);
  const u64 t_start = __builtin_amdgcn_s_memrealtime();
  if (tid < 16) flag[tid] = 0u;
  asm volatile("" :: "s"(x), "s"(kc), "s"(vc), "s"(wq), "s"(wo), "s"(wg), "s"(wu), "s"(wd), "s"(out), "s"(ws));
  u64 epoch_r = 0; unsigned thi_r = 0, tlo_r = 0;

  if (wv == 0) {
    STAMP(0);
    __builtin_amdgcn_s_setprio(3);
    v2u xr[5];
#pragma unroll
    for (int t = 0; t < 5; ++t) xr[t] = *(const GAS v2u*)(x + (lane >> 5) * D + t * 128 + (lane & 31) * 4);
    v4u rq[20];
    const __amdgpu_buffer_rsrc_t rq_rs = __builtin_amdgcn_make_buffer_rsrc((void*)(wq + (size_t)g8 * 80 * (H * DH) + idx * DH), 0, (79 * H * DH + DH) * 2, 0x00020000);
#pragma unroll
    for (int i = 0; i < 20; ++i) rq[i] = __builtin_amdgcn_raw_buffer_load_b128(rq_rs, (unsigned)(4 * g * (H * DH) + cs * 8) * 2u, ((i >> 2) * 16 + (i & 3)) * (H * DH) * 2, NT_W ? 2 : 0);
    SCHED();
    BAR();
    SCHED();
    TAGS();
    float xv[20];
#pragma unroll
    for (int t = 0; t < 5; ++t) { xv[4 * t] = lo2f(xr[t].x); xv[4 * t + 1] = hi2f(xr[t].x); xv[4 * t + 2] = lo2f(xr[t].y); xv[4 * t + 3] = hi2f(xr[t].y); }
    float sm = 0.f;
#pragma unroll
    for (int i = 0; i < 20; ++i) sm += xv[i];
    const float mean = sumx16(sum16(sm)) * (1.f / D);
    float sq = 0.f;
#pragma unroll
    for (int i = 0; i < 20; ++i) { const float d = xv[i] - mean; sq = fmaf(d, d, sq); }
    const float var = rbf(sumx16(sum16(sq)) * (1.f / D)), mb = rbf(mean);
    const float rden = __builtin_amdgcn_rcpf(rbf(__builtin_amdgcn_sqrtf(rbf(var + 1e-5f))));
#pragma unroll
    for (int t = 0; t < 5; ++t) {
      const unsigned d0 = cvtpk(xv[4 * t] - mb, xv[4 * t + 1] - mb), d1 = cvtpk(xv[4 * t + 2] - mb, xv[4 * t + 3] - mb);
      *(LAS v2u*)(L + L_XN + ((lane >> 5) * D + t * 128 + (lane & 31) * 4) * 2) = v2u{cvtpk(lo2f(d0) * rden, hi2f(d0) * rden), cvtpk(lo2f(d1) * rden, hi2f(d1) * rden)};
    }
    LDS_WAIT();
    v2u af[5];
#pragma unroll
    for (int t = 0; t < 5; ++t) af[t] = *(const LAS v2u*)(L + L_XN + ((lane & 1) * D + g8 * 80 + t * 16 + 4 * g) * 2);
    f32x4 acc[8];
#pragma unroll
    for (int c = 0; c < 8; ++c) acc[c] = f32x4{0.f, 0.f, 0.f, 0.f};
#pragma unroll
    for (int t = 0; t < 5; ++t) {
      v2u bt[8];
      tr4(rq + 4 * t, bt);
#pragma unroll
      for (int c = 0; c < 8; ++c) acc[c] = mfma16(af[t], bt[c], acc[c]);
    }
#pragma unroll
    for (int bb = 0; bb < 2; ++bb) {
      const float v0 = sel4(g, acc[0][bb], acc[2][bb], acc[4][bb], acc[6][bb]), v1 = sel4(g, acc[1][bb], acc[3][bb], acc[5][bb], acc[7][bb]);
      st_sc1(rs, WS_QP + (unsigned)(((idx * 2 + bb) * 8 + g8) * 128 + cs * 8 + 2 * g) * 8u, v4u{pack24(v0, tlo), thi, pack24(v1, tlo), thi});
    }
    STAMP(1);
    {
      v4u qg[8];
      const unsigned off = WS_QP + (unsigned)((h * 2 + b) * 8 * 128) * 8u + (unsigned)lane * 16u;
      _Pragma("nounroll") for (unsigned n = 0; n < SPIN_G; ++n) {
        unsigned bad = 0u;
#pragma unroll
        for (int i = 0; i < 8; ++i) qg[i] = ld_sc1(rs, off + i * 1024u);
#pragma unroll
        for (int i = 0; i < 8; ++i) bad |= bad24(qg[i], tlo, thi);
        if (__all(bad == 0u)) break;
        MEMBAR();
      }
      float q0 = 0.f, q1 = 0.f;
#pragma unroll
      for (int i = 0; i < 8; ++i) { q0 += unpack24(qg[i].x); q1 += unpack24(qg[i].z); }
      *(LAS unsigned*)(L + L_Q + lane * 4) = cvtpk(q0, q1);
      LDS_WAIT();
      flag[0] = 1u;
    }
    STAMP(2);
    lds_wait_ge(flag + 1, 4u);
    {
      v4u sg[8];
      const unsigned off = WS_SC + (unsigned)((b * H + h) * W) * 8u + (unsigned)lane * 16u;
      _Pragma("nounroll") for (unsigned n = 0; n < SPIN_G; ++n) {
        unsigned bad = 0u;
#pragma unroll
        for (int i = 0; i < 8; ++i) sg[i] = ld_sc1(rs, off + i * 1024u);
#pragma unroll
        for (int i = 0; i < 8; ++i) bad |= (((sg[i].x ^ t16) | (sg[i].z ^ t16)) & 0xffffu) | (sg[i].y ^ thi) | (sg[i].w ^ thi);
        if (__all(bad == 0u)) break;
        MEMBAR();
      }
      STAMP(4);
      float sv[16], m = -INFINITY;
#pragma unroll
      for (int i = 0; i < 8; ++i) { sv[2 * i] = hi2f(sg[i].x); sv[2 * i + 1] = hi2f(sg[i].z); m = fmaxf(m, fmaxf(sv[2 * i], sv[2 * i + 1])); }
      m = maxx32(maxx16(max16(m)));
      float l = 0.f;
#pragma unroll
      for (int i = 0; i < 16; ++i) { sv[i] = __builtin_amdgcn_exp2f((sv[i] - m) * 1.4426950408889634f); l += sv[i]; }
      const float rl = __builtin_amdgcn_rcpf(sumx32(sumx16(sum16(l))));
#pragma unroll
      for (int i = 0; i < 8; ++i) *(LAS unsigned*)(L + L_P + (i * 128 + lane * 2) * 2) = cvtpk(sv[2 * i] * rl, sv[2 * i + 1] * rl);
      LDS_WAIT();
      flag[2] = 1u;
    }
    STAMP(5);
    return;
  }

  if (wv <= 4) {
    __builtin_amdgcn_s_setprio(2);
    const int kw = wv - 1;
    const u16* kb = kc + ((size_t)(b * H + h) * SK + (SK - W) + s * 256 + kw * 64 + g) * DH + cs * 8;
    v4u rk[16];
#pragma unroll
    for (int i = 0; i < 4; ++i) rk[i] = ldg<NT_KV>(kb + (size_t)i * 4 * DH);
    SCHED();
    BAR();
#pragma unroll
    for (int i = 4; i < 16; ++i) rk[i] = ldg<NT_KV>(kb + (size_t)i * 4 * DH);
    SCHED();
    TAGS();
    epoch_r = epoch; thi_r = thi; tlo_r = tlo;
    lds_wait_ge(flag + 0, 1u);
    const v4u qf = *(const LAS v4u*)(L + L_Q + cs * 16);
    float xs[16];
#pragma unroll
    for (int i = 0; i < 16; ++i) xs[i] = dot2(rk[i].w, qf.w, dot2(rk[i].z, qf.z, dot2(rk[i].y, qf.y, dot2(rk[i].x, qf.x, 0.f))));
#define TR_STEP(HALF, CTRL, BIT) _Pragma("unroll") for (int i = 0; i < (HALF); ++i) { \
      const float t1 = xs[i] + dpp<CTRL>(xs[i]), t2 = xs[i + (HALF)] + dpp<CTRL>(xs[i + (HALF)]); xs[i] = (cs & (BIT)) ? t2 : t1; }
    TR_STEP(8, XOR8, 8) TR_STEP(4, XOR7, 4) TR_STEP(2, XOR2, 2) TR_STEP(1, XOR1, 1)
#undef TR_STEP
    const float sc = rbf(xs[0]) * 0.08838834764831845f;
    st_sc1_64(rs, WS_SC + (unsigned)((b * H + h) * W + s * 256 + kw * 64 + 4 * cs + g) * 8u, v2u{(cvtpk(sc, sc) & 0xffff0000u) | t16, thi});
    if (lane == 0) __hip_atomic_fetch_add((LAS unsigned*)(L + L_FLAG) + 1, 1u, __ATOMIC_RELAXED, __HIP_MEMORY_SCOPE_WORKGROUP);
    if (wv == 1) STAMP(3);
    if (!is_red) return;
    BAR();
  } else if (wv <= 8) {
    __builtin_amdgcn_s_setprio(1);
    BAR();
    const int vw = wv - 5;
    const u16* vb = vc + ((size_t)(b * H + h) * SK + (SK - W) + vw * 256 + (cs >> 2) * 32 + g * 8) * DH + s * 32 + (cs & 3) * 8;
    v4u rv[16];
#pragma unroll
    for (int i = 0; i < 16; ++i) rv[i] = ldg<NT_KV>(vb + (size_t)((i >> 3) * 128 + (i & 7)) * DH);
    SCHED();
    v4u bt[16];
    TAGS();
    epoch_r = epoch; thi_r = thi; tlo_r = tlo;
    tr8(rv, bt); tr8(rv + 8, bt + 8);
    PIN8(bt); PIN8(bt + 8);
    if (wv == 5) STAMP(6);
    lds_wait_ge(flag + 2, 1u);
    const v4u p0 = *(const LAS v4u*)(L + L_P + (vw * 256 + (cs >> 2) * 32 + g * 8) * 2);
    const v4u p1 = *(const LAS v4u*)(L + L_P + (vw * 256 + 128 + (cs >> 2) * 32 + g * 8) * 2);
    f32x4 acc[8];
#pragma unroll
    for (int c = 0; c < 8; ++c) acc[c] = mfma32(p1, bt[8 + c], mfma32(p0, bt[c], f32x4{0.f, 0.f, 0.f, 0.f}));
    if ((cs >> 2) == g) {
      LAS f32x4* o = (LAS f32x4*)(L + L_OP + ((vw * 4 + g) * 32 + (cs & 3) * 8) * 4);
      o[0] = f32x4{acc[0][0], acc[1][0], acc[2][0], acc[3][0]};
      o[1] = f32x4{acc[4][0], acc[5][0], acc[6][0], acc[7][0]};
    }
    BAR();
    if (!is_red) return;
  } else {
    const int ww = wv - 9;
    BAR();
    v4u ro[8], rg[8], rd[4];
    const u16* ob = wo + (size_t)(h * DH + s * 32 + g * 8) * D + ww * 128 + cs * 8;
#pragma unroll
    for (int i = 0; i < 8; ++i) ro[i] = ldg<NT_W>(ob + (size_t)i * D);
    SCHED();
    const u16* gb = ((cs & 2) ? wu : wg) + (size_t)(ww * 128 + (cs >> 2) * 32 + g * 8) * F + f * 16 + (cs & 1) * 8;
    const u16* db = wd + (size_t)(f * 16 + 4 * g) * D + ww * 128 + cs * 8;
    TAGS();
    v4u bo[8];
    tr8(ro, bo);
    PIN8(bo);
    if (is_ffn) {
#pragma unroll
      for (int i = 0; i < 8; ++i) rg[i] = ldg<NT_W>(gb + (size_t)i * F);
#pragma unroll
      for (int i = 0; i < 4; ++i) rd[i] = ldg<NT_W>(db + (size_t)i * D);
    }
    SCHED();
    if (wv == 9) STAMP(7);
    BAR();
    if (wv == 9) STAMP(8);
    {
      const LAS f32x4* op = (const LAS f32x4*)(L + L_OP + (cs * 32 + g * 8) * 4);
      const f32x4 a0 = op[0], a1 = op[1];
      const v4u af = {cvtpk(sum16(a0[0]), sum16(a0[1])), cvtpk(sum16(a0[2]), sum16(a0[3])), cvtpk(sum16(a1[0]), sum16(a1[1])), cvtpk(sum16(a1[2]), sum16(a1[3]))};
      f32x4 acc[8];
#pragma unroll
      for (int c = 0; c < 8; ++c) acc[c] = mfma32(af, bo[c], f32x4{0.f, 0.f, 0.f, 0.f});
      const float v0 = sel4(g, acc[0][0], acc[2][0], acc[4][0], acc[6][0]), v1 = sel4(g, acc[1][0], acc[3][0], acc[5][0], acc[7][0]);
      const int col = ww * 128 + cs * 8 + 2 * g;
      st_sc1(rs, WS_T1 + (unsigned)(((b * NCG + col / RC) * 128 + h * 4 + s) * RC + col % RC) * 8u, v4u{pack24(v0, tlo), thi, pack24(v1, tlo), thi});
    }
    if (wv == 9) STAMP(9);
    if (!is_ffn) return;
    v4u bg[8]; v2u bd[8];
    tr8(rg, bg); tr4(rd, bd);
    PIN8(bg); PIN8(bd);
    v4u hg;
    {
      const unsigned off = WS_HB + (unsigned)((lane >> 5) * 320 + ww * 64 + (lane & 31) * 2) * 8u;
      _Pragma("nounroll") for (unsigned n = 0; n < SPIN_G; ++n) {
        hg = ld_sc1(rs, off);
        if (__all(((hg.y ^ thi) | (hg.w ^ thi)) == 0u)) break;
        MEMBAR();
      }
    }
    if (wv == 9) STAMP(11);
    const float h0 = lo2f(hg.x), h1 = hi2f(hg.x), h2 = lo2f(hg.z), h3 = hi2f(hg.z);
    const float ls = sumx16(sum16((h0 + h1) + (h2 + h3)));
    const float lm = ls * (1.f / 128.f);
    const float e0 = h0 - lm, e1 = h1 - lm, e2 = h2 - lm, e3 = h3 - lm;
    const float lq = sumx16(sum16(fmaf(e0, e0, e1 * e1) + fmaf(e2, e2, e3 * e3)));
    if ((lane & 31) == 0) *(LAS f32x2*)(L + L_ST + (ww * 2 + (lane >> 5)) * 8) = f32x2{ls, lq};
    BAR();
    f32x2 st[5];
#pragma unroll
    for (int i = 0; i < 5; ++i) st[i] = *(const LAS f32x2*)(L + L_ST + (i * 2 + (lane >> 5)) * 8);
    const float mean = (((st[0][0] + st[1][0]) + (st[2][0] + st[3][0])) + st[4][0]) * (1.f / D);
    float m2 = 0.f;
#pragma unroll
    for (int i = 0; i < 5; ++i) { const float d = st[i][0] * (1.f / 128.f) - mean; m2 += fmaf(128.f * d, d, st[i][1]); }
    const float var = rbf(m2 * (1.f / D)), mb = rbf(mean);
    const float rden = __builtin_amdgcn_rcpf(rbf(__builtin_amdgcn_sqrtf(rbf(var + 1e-5f))));
    const unsigned d0 = cvtpk(h0 - mb, h1 - mb), d1 = cvtpk(h2 - mb, h3 - mb);
    *(LAS v2u*)(L + L_XN + ((lane >> 5) * D + ww * 128 + (lane & 31) * 4) * 2) = v2u{cvtpk(lo2f(d0) * rden, hi2f(d0) * rden), cvtpk(lo2f(d1) * rden, hi2f(d1) * rden)};
    LDS_WAIT();
    {
      const v4u af = *(const LAS v4u*)(L + L_XN + ((lane & 1) * D + ww * 128 + (cs >> 2) * 32 + g * 8) * 2);
      f32x4 acc[8];
#pragma unroll
      for (int c = 0; c < 8; ++c) acc[c] = mfma32(af, bg[c], f32x4{0.f, 0.f, 0.f, 0.f});
      if ((cs >> 2) == g) {
#pragma unroll
        for (int bb = 0; bb < 2; ++bb) {
          LAS f32x4* o = (LAS f32x4*)(L + L_GU + ((ww * 4 + g) * 64 + ((cs >> 1) & 1) * 32 + bb * 16 + (cs & 1) * 8) * 4);
          o[0] = f32x4{acc[0][bb], acc[1][bb], acc[2][bb], acc[3][bb]};
          o[1] = f32x4{acc[4][bb], acc[5][bb], acc[6][bb], acc[7][bb]};
        }
      }
    }
    BAR();
    {
      const LAS float* gu = (const LAS float*)(L + L_GU) + lane;
      float t[20];
#pragma unroll
      for (int i = 0; i < 20; ++i) t[i] = gu[i * 64];
      float a = 0.f;
#pragma unroll
      for (int i = 0; i < 5; ++i) a += (t[4 * i] + t[4 * i + 1]) + (t[4 * i + 2] + t[4 * i + 3]);
      a = rbf(a);
      auto sw = __builtin_amdgcn_permlane32_swap(__float_as_uint(a), __float_as_uint(a), false, false);
      const float gv = __uint_as_float(sw[0]), uv = __uint_as_float(sw[1]);
      const float sg = rbf(gv * __builtin_amdgcn_rcpf(1.f + __builtin_amdgcn_exp2f(gv * -1.4426950408889634f)));
      const unsigned av = cvtpk(sg * uv, 0.f);
      if (lane < 32) *(LAS u16*)(L + L_ACT + ww * 64 + lane * 2) = (u16)av;
      LDS_WAIT();
      const v2u af = *(const LAS v2u*)(L + L_ACT + ww * 64 + (lane & 1) * 32 + g * 8);
      f32x4 acc[8];
#pragma unroll
      for (int c = 0; c < 8; ++c) acc[c] = mfma16(af, bd[c], f32x4{0.f, 0.f, 0.f, 0.f});
      const int col = ww * 128 + cs * 8 + 2 * g;
#pragma unroll
      for (int bb = 0; bb < 2; ++bb) {
        const float v0 = sel4(g, acc[0][bb], acc[2][bb], acc[4][bb], acc[6][bb]), v1 = sel4(g, acc[1][bb], acc[3][bb], acc[5][bb], acc[7][bb]);
        st_sc1(rs, WS_T3 + (unsigned)(((bb * NCG + col / RC) * 128 + f) * RC + col % RC) * 8u, v4u{pack24(v0, tlo), thi, pack24(v1, tlo), thi});
      }
    }
    if (wv == 9) STAMP(12);
    return;
  }

  const int rw = wv - 1, rb = r / NCG, rc0 = (r % NCG) * RC;
  const u64 epoch = epoch_r; const unsigned thi = thi_r, tlo = tlo_r;
  const float xv = lo2f(x[rb * D + rc0 + cs]);
  const float a1 = reduce_inbox(rs, WS_T1 + (unsigned)r * (128u * RC * 8u), 128, rw, lane, tlo, thi, (LAS float*)(L + L_RED));
  const float hv = rbf(xv + rbf(a1));
  if (rw == 0) {
    const float hn = dpp<XOR1>(hv);
    if (lane < 16 && !(lane & 1)) st_sc1_64(rs, WS_HB + (unsigned)(rb * 320 + (rc0 + lane) / 2) * 8u, v2u{cvtpk(hv, hn), thi});
    STAMP(10);
  }
  const float a2 = reduce_inbox(rs, WS_T3 + (unsigned)r * (128u * RC * 8u), NFFN, rw, lane, tlo, thi, (LAS float*)(L + L_RED + 512));
  if (rw == 0) {
    const float ov = hv + rbf(a2);
    const float on = dpp<XOR1>(ov);
    if (lane < 16 && !(lane & 1)) *(GAS unsigned*)(out + rb * D + rc0 + lane) = cvtpk(ov, on);
    if (r == 0 && lane == 0) *(GAS u64*)(ws + WS_EPOCH) = epoch;
    STAMP(13);
    while (__builtin_amdgcn_s_memrealtime() - t_start < PADT) __builtin_amdgcn_s_sleep(1);
  }
}

extern "C" void kernel_launch(void* const* d_in, const int* in_sizes, int n_in, void* d_out, int out_size, void* d_ws, size_t ws_size, hipStream_t stream) {
  (void)hipFuncSetAttribute((const void*)fused_block, hipFuncAttributeMaxDynamicSharedMemorySize, LDS_BYTES);
  fused_block<<<NWG, NTHR, LDS_BYTES, stream>>>((const u16*)d_in[0], (const u16*)d_in[1], (const u16*)d_in[2], (const u16*)d_in[3], (const u16*)d_in[4],
                                               (const u16*)d_in[6], (const u16*)d_in[7], (const u16*)d_in[8], (u16*)d_out, (unsigned char*)d_ws);
}
```

```cpp
#include <hip/hip_runtime.h>
#include <hip/hip_bf16.h>

#define NT_KV 1
#define NT_W 1
#define NT_GU 0
#define WN 2
#define SCORE_AT 8
#define WO_AT 8
#define GU_AT 9
#define GW 16
#define T4_DELAY 24
#define CLR_DELAY 0
#define FL_AT F_WO
#define Q_DELAY 32
#define T2_DELAY 32
#define PREF 3
#define PW 4
#define GU_DELAY 40
#define PREF_NT 0
#define KV_LIN 1
#define WN2 8
#define P1_LATE 1
#define P2_LATE 1
#define DYN 1
#define HREP 1
#define H_DELAY 0
#define RED0 64
#define KV_GATE 1
#define STAMP_OFF (64u << 20)
#define NSTAMP 24

typedef unsigned short u16;
typedef unsigned long long u64;
typedef unsigned v4u __attribute__((ext_vector_type(4)));
typedef unsigned v2u __attribute__((ext_vector_type(2)));
typedef float f32x4 __attribute__((ext_vector_type(4)));
typedef float f32x2 __attribute__((ext_vector_type(2)));
typedef __bf16 bf16x2 __attribute__((ext_vector_type(2)));
typedef __bf16 bf16x8 __attribute__((ext_vector_type(8)));
typedef short s16x4 __attribute__((ext_vector_type(4)));
#define GAS __attribute__((address_space(1)))
#define LAS __attribute__((address_space(3)))
#define CAS __attribute__((address_space(4)))

constexpr int D = 640, H = 32, DH = 128, SK = 4096, W = 1024, F = 1728;
constexpr int NWG = 256, NWAVE = 14, NTHR = NWAVE * 64;
constexpr int NFFN = 54;
constexpr int NRED = 40;
constexpr unsigned SPIN_G = 200000, SPIN_L = 4000000;
constexpr unsigned EMPTY = 0xffffffffu;

constexpr unsigned WS_EPOCH = 0;
constexpr unsigned WS_FL = 4096;
constexpr unsigned WS_HB = WS_FL + 4096;
constexpr unsigned HB_REP = 4096;
constexpr unsigned WS_OP = WS_HB + 8 * HB_REP;
constexpr unsigned WS_QP = WS_OP + H * 4 * 1024;
constexpr unsigned WS_T1 = WS_QP + H * 2 * 8 * 128 * 8;
constexpr unsigned WS_T3 = WS_T1 + NRED * 128 * 128;
constexpr unsigned WS_END = WS_T3 + NRED * 128 * 128;

enum { F_Q = 0, F_WO, F_ML, F_OP, F_OPUB, F_GU, F_CLR, F_ALL, F_T1, F_R1, F_R2, F_GO, F_QPUB, F_QC };
constexpr int L_FLAG = 0;
constexpr int L_XN = 64;
constexpr int L_Q = L_XN + 2560;
constexpr int L_ML = L_Q + 256;
constexpr int L_OP = L_ML + 64;
constexpr int L_ST = L_OP + 4096;
constexpr int L_GU = L_ST + 64;
constexpr int L_RED = L_GU;
constexpr int L_HV = L_RED + 2048;
constexpr int L_QP = L_GU;
constexpr int L_END = L_GU + 5120 + 8192;
constexpr int LDS_BYTES = 24 * 1024;
static_assert(L_END <= LDS_BYTES, "LDS map");

template <int NT> __device__ __forceinline__ v4u ldg(const u16* p) {
  if (NT) return __builtin_nontemporal_load((const GAS v4u*)p);
  return *(const GAS v4u*)p;
}
__device__ __forceinline__ unsigned cvtpk(float lo, float hi) {
  asm("" : "+v"(lo));
  asm("" : "+v"(hi));
  f32x2 v = {lo, hi};
  return __builtin_bit_cast(unsigned, __builtin_convertvector(v, bf16x2));
}
__device__ __forceinline__ float lo2f(unsigned u) { return __uint_as_float(u << 16); }
__device__ __forceinline__ float hi2f(unsigned u) { return __uint_as_float(u & 0xffff0000u); }
__device__ __forceinline__ float rbf(float f) { return lo2f(cvtpk(f, f)); }
__device__ __forceinline__ float vmax(float a, float b) { float r; asm("v_max_f32 %0, %1, %2" : "=v"(r) : "v"(a), "v"(b)); return r; }
__device__ __forceinline__ float dot2(unsigned a, unsigned b, float c) {
  return __builtin_amdgcn_fdot2_f32_bf16(__builtin_bit_cast(bf16x2, a), __builtin_bit_cast(bf16x2, b), c, false);
}
template <int CTRL> __device__ __forceinline__ float dpp(float x) {
  return __builtin_bit_cast(float, __builtin_amdgcn_mov_dpp(__builtin_bit_cast(int, x), CTRL, 0xf, 0xf, true));
}
constexpr int XOR1 = 0xB1, XOR2 = 0x4E, XOR7 = 0x141, XOR8 = 0x128, ROR4 = 0x124;
__device__ __forceinline__ float sum16(float x) { x += dpp<XOR1>(x); x += dpp<XOR2>(x); x += dpp<XOR7>(x); x += dpp<XOR8>(x); return x; }
__device__ __forceinline__ float max16(float x) { x = fmaxf(x, dpp<XOR1>(x)); x = fmaxf(x, dpp<XOR2>(x)); x = fmaxf(x, dpp<XOR7>(x)); x = fmaxf(x, dpp<XOR8>(x)); return x; }
__device__ __forceinline__ float sumx16(float x) {
  auto s = __builtin_amdgcn_permlane16_swap(__float_as_uint(x), __float_as_uint(x), false, false);
  return __uint_as_float(s[0]) + __uint_as_float(s[1]);
}
__device__ __forceinline__ float sumx32(float x) {
  auto s = __builtin_amdgcn_permlane32_swap(__float_as_uint(x), __float_as_uint(x), false, false);
  return __uint_as_float(s[0]) + __uint_as_float(s[1]);
}
__device__ __forceinline__ float maxx16(float x) {
  auto s = __builtin_amdgcn_permlane16_swap(__float_as_uint(x), __float_as_uint(x), false, false);
  return vmax(__uint_as_float(s[0]), __uint_as_float(s[1]));
}
__device__ __forceinline__ float maxx32(float x) {
  auto s = __builtin_amdgcn_permlane32_swap(__float_as_uint(x), __float_as_uint(x), false, false);
  return vmax(__uint_as_float(s[0]), __uint_as_float(s[1]));
}
__device__ __forceinline__ f32x4 mfma32(v4u a, v4u b, f32x4 c) {
  return __builtin_amdgcn_mfma_f32_16x16x32_bf16(__builtin_bit_cast(bf16x8, a), __builtin_bit_cast(bf16x8, b), c, 0, 0, 0);
}
__device__ __forceinline__ f32x4 mfma16(v2u a, v2u b, f32x4 c) {
  return __builtin_amdgcn_mfma_f32_16x16x16bf16_1k(__builtin_bit_cast(s16x4, a), __builtin_bit_cast(s16x4, b), c, 0, 0, 0);
}
__device__ __forceinline__ void tr8(const v4u* r, v4u* t) {
#pragma unroll
  for (int dw = 0; dw < 4; ++dw)
#pragma unroll
    for (int pr = 0; pr < 4; ++pr) {
      t[2 * dw][pr] = __builtin_amdgcn_perm(r[2 * pr + 1][dw], r[2 * pr][dw], 0x05040100u);
      t[2 * dw + 1][pr] = __builtin_amdgcn_perm(r[2 * pr + 1][dw], r[2 * pr][dw], 0x07060302u);
    }
}
__device__ __forceinline__ void tr4(const v4u* r, v2u* t) {
#pragma unroll
  for (int dw = 0; dw < 4; ++dw)
#pragma unroll
    for (int pr = 0; pr < 2; ++pr) {
      t[2 * dw][pr] = __builtin_amdgcn_perm(r[2 * pr + 1][dw], r[2 * pr][dw], 0x05040100u);
      t[2 * dw + 1][pr] = __builtin_amdgcn_perm(r[2 * pr + 1][dw], r[2 * pr][dw], 0x07060302u);
    }
}
__device__ __forceinline__ float sel4(int g, float a, float b, float c, float d) {
  const float x = (g & 1) ? b : a, y = (g & 1) ? d : c;
  return (g & 2) ? y : x;
}
__device__ __forceinline__ unsigned pack24(float v, unsigned tlo) { return ((__float_as_uint(v) + 0x80u) & 0xffffff00u) | tlo; }
__device__ __forceinline__ float unpack24(unsigned u) { return __uint_as_float(u & 0xffffff00u); }
__device__ __forceinline__ unsigned bad24(v4u g, unsigned tlo, unsigned thi) {
  return (((g.x ^ tlo) | (g.z ^ tlo)) & 0xffu) | (g.y ^ thi) | (g.w ^ thi);
}
constexpr int SC1 = 16;
__device__ __forceinline__ v4u ld_sc1(__amdgpu_buffer_rsrc_t rs, unsigned off) { return __builtin_amdgcn_raw_buffer_load_b128(rs, off, 0, SC1); }
__device__ __forceinline__ void st_sc1(__amdgpu_buffer_rsrc_t rs, unsigned off, v4u v) { __builtin_amdgcn_raw_buffer_store_b128(v, rs, off, 0, SC1); }
__device__ __forceinline__ void st_sc1_64(__amdgpu_buffer_rsrc_t rs, unsigned off, v2u v) { __builtin_amdgcn_raw_buffer_store_b64(v, rs, off, 0, SC1); }
#define MEMBAR() asm volatile("" ::: "memory")
#define LDS_WAIT() asm volatile("s_waitcnt lgkmcnt(0)" ::: "memory")
#define SCHED() __builtin_amdgcn_sched_barrier(0)
#define BAR() do { LDS_WAIT(); __builtin_amdgcn_s_barrier(); MEMBAR(); } while (0)
__device__ __forceinline__ void lds_wait_ge(volatile LAS unsigned* f, unsigned want) {
  for (unsigned n = 0; *f < want && n < SPIN_L; ++n) __builtin_amdgcn_s_sleep(1);
  MEMBAR();
}
__device__ __forceinline__ u64 load_epoch(const unsigned char* ws) {
  u64 e;
  asm volatile("s_load_dwordx2 %0, %1, 0x0\n\ts_waitcnt lgkmcnt(0)" : "=s"(e) : "s"(ws) : "memory");
  return e + 1ull;
}
#define TAGS() const u64 epoch = load_epoch(ws); SCHED(); const u64 tag = epoch * 0x9E3779B97F4A7C15ull; \
  unsigned thi = (unsigned)tag, tlo = (unsigned)(tag >> 32) & 0xffu; asm volatile("" : "+s"(thi), "+s"(tlo))
#define PIN4(a) asm volatile("" : "+v"((a)[0]), "+v"((a)[1]), "+v"((a)[2]), "+v"((a)[3]))
#define PIN8(a) do { PIN4(a); PIN4((a) + 4); } while (0)
#define STAMP(k) do {} while (0)
#define STAMP_FLUSH() do {} while (0)

#define VMWAIT(n) asm volatile("s_waitcnt vmcnt(%0)" :: "n"(n) : "memory")
#define KEEP8(a) asm volatile("" :: "v"((a)[0]), "v"((a)[1]), "v"((a)[2]), "v"((a)[3]), "v"((a)[4]), "v"((a)[5]), "v"((a)[6]), "v"((a)[7]))
__device__ __forceinline__ void lds_inc(LAS unsigned char* L, int i) {
  asm volatile("ds_add_u32 %0, %1" :: "v"((unsigned)(size_t)(L + L_FLAG + 4 * i)), "v"(1u) : "memory");
}
__device__ __forceinline__ unsigned lds_inc_rtn(LAS unsigned char* L, int i) {
  unsigned r;
  asm volatile("ds_add_rtn_u32 %0, %1, %2\n\ts_waitcnt lgkmcnt(0)" : "=v"(r) : "v"((unsigned)(size_t)(L + L_FLAG + 4 * i)), "v"(1u) : "memory");
  return r;
}
__device__ __forceinline__ void tr_pair(const v4u& r0, const v4u& r1, v4u* t, int pr) {
#pragma unroll
  for (int dw = 0; dw < 4; ++dw) {
    t[2 * dw][pr] = __builtin_amdgcn_perm(r1[dw], r0[dw], 0x05040100u);
    t[2 * dw + 1][pr] = __builtin_amdgcn_perm(r1[dw], r0[dw], 0x07060302u);
  }
}
__device__ __forceinline__ unsigned ldp(const GAS unsigned* p) { return PREF_NT ? __builtin_nontemporal_load(p) : *p; }
__device__ __forceinline__ unsigned umax3(unsigned a, unsigned b, unsigned c) { return max(max(a, b), c); }
__device__ __forceinline__ bool has_empty(v4u a) { return umax3(umax3(a.x, a.y, a.z), a.w, 0u) == EMPTY; }
__device__ __forceinline__ unsigned enc(float v) { return min(__float_as_uint(v), EMPTY - 1u); }
__device__ __forceinline__ void lds_spin_ge(volatile LAS unsigned* f, unsigned want) {
  for (unsigned n = 0; *f < want && n < SPIN_L; ++n) {}
  MEMBAR();
}
__device__ __forceinline__ unsigned tile_off(unsigned base, int prod, int tile, int lane) {
  return base + (unsigned)(tile * 8 + (lane >> 3)) * 16384u + (unsigned)(prod * 128 + (lane & 7) * 16);
}
__device__ __forceinline__ void publish_tile(__amdgpu_buffer_rsrc_t rs, unsigned off, int g, const f32x4* acc) {
  const bool up = g & 1;
  st_sc1(rs, off, v4u{enc(up ? acc[4][0] : acc[0][0]), enc(up ? acc[5][0] : acc[1][0]), enc(up ? acc[6][0] : acc[2][0]), enc(up ? acc[7][0] : acc[3][0])});
}
__device__ __forceinline__ v4u join_quads(unsigned a, unsigned b) {
  return v4u{(unsigned)__builtin_amdgcn_update_dpp((int)a, (int)a, XOR7, 0xf, 0xa, false), (unsigned)__builtin_amdgcn_update_dpp((int)b, (int)b, XOR7, 0xf, 0xa, false),
             (unsigned)__builtin_amdgcn_update_dpp((int)a, (int)a, XOR7, 0xf, 0x5, false), (unsigned)__builtin_amdgcn_update_dpp((int)b, (int)b, XOR7, 0xf, 0x5, false)};
}
template <int NP> __device__ __forceinline__ bool reduce_inbox(__amdgpu_buffer_rsrc_t rs, unsigned base, int rw, int lane, LAS unsigned char* L, int red_off, int cnt, float& tot, unsigned& tpoll) {
  constexpr bool TWO = NP > 64;
  const unsigned off = (TWO || rw * 8 + (lane >> 3) < NP) ? base + (unsigned)rw * (TWO ? 2048u : 1024u) + (unsigned)lane * 16u : WS_END;
  v4u a, b = {0u, 0u, 0u, 0u};
  _Pragma("nounroll") for (unsigned n = 0; n < SPIN_G; ++n) {
    a = ld_sc1(rs, off);
    if (TWO) b = ld_sc1(rs, off + 1024u);
    if (!__any(has_empty(a) || has_empty(b))) break;
    MEMBAR();
  }
  float v0 = __uint_as_float(a.x), v1 = __uint_as_float(a.y), v2 = __uint_as_float(a.z), v3 = __uint_as_float(a.w);
  if (TWO) { v0 += __uint_as_float(b.x); v1 += __uint_as_float(b.y); v2 += __uint_as_float(b.z); v3 += __uint_as_float(b.w); }
  const auto s0 = __builtin_amdgcn_permlane32_swap(__float_as_uint(v0), __float_as_uint(v2), false, false);
  const auto s1 = __builtin_amdgcn_permlane32_swap(__float_as_uint(v1), __float_as_uint(v3), false, false);
  const float u0 = __uint_as_float(s0[0]) + __uint_as_float(s0[1]), u1 = __uint_as_float(s1[0]) + __uint_as_float(s1[1]);
  const auto s2 = __builtin_amdgcn_permlane16_swap(__float_as_uint(u0), __float_as_uint(u1), false, false);
  float t = __uint_as_float(s2[0]) + __uint_as_float(s2[1]);
  t += dpp<XOR8>(t);
  const int slot = (lane >> 4) * 8 + (lane & 7);
  LAS float* red = (LAS float*)(L + red_off);
  red[rw * 32 + slot] = t;
  unsigned old = 0;
  if (lane == 0) old = lds_inc_rtn(L, cnt);
  if (__builtin_amdgcn_readfirstlane(old) != 7u) return false;
  MEMBAR();
  const LAS float* rp = red + ((lane >> 3) & 1) * 128 + slot;
  const float p = (rp[0] + rp[32]) + (rp[64] + rp[96]);
  tot = p + dpp<XOR8>(p);
  return true;
}

__global__ void __launch_bounds__(NTHR, 4) fused_block(const u16* __restrict__ x, const u16* __restrict__ kc, const u16* __restrict__ vc, const u16* __restrict__ wq,
                                                       const u16* __restrict__ wo, const u16* __restrict__ wg, const u16* __restrict__ wu, const u16* __restrict__ wd,
                                                       u16* __restrict__ out, unsigned char* ws) {
  __shared__ __attribute__((aligned(16))) unsigned char lds_raw[LDS_BYTES];
  LAS unsigned char* L = (LAS unsigned char*)lds_raw;
  volatile LAS unsigned* flag = (volatile LAS unsigned*)(L + L_FLAG);
  const int tid = threadIdx.x, lane = tid & 63, wv = __builtin_amdgcn_readfirstlane(tid >> 6), w = blockIdx.x;
  const int g = lane >> 4, cs = lane & 15;
  const int g8 = w & 7, idx = w >> 3, h = g8 + 8 * (idx & 3), j = idx >> 2, b = j & 1, s = j >> 1;
  const int t1 = w - 128;
  const int rd_ = w - RED0;
  const int lg0 = (g8 * 27) >> 3, nlg = (((g8 + 1) * 27) >> 3) - lg0;
  const int f = 2 * lg0 + idx;
  const bool is_ffn = idx < 2 * nlg, is_red = rd_ >= 0 && rd_ < NRED;
  unsigned stv = 0; (void)stv;
  if (wv == 0) STAMP(0);
  if (tid < 16) flag[tid] = 0u;
  BAR();
  asm volatile("" :: "s"(x), "s"(kc), "s"(vc), "s"(wq), "s"(wo), "s"(wg), "s"(wu), "s"(wd), "s"(out), "s"(ws));
  const __amdgpu_buffer_rsrc_t rs = __builtin_amdgcn_make_buffer_rsrc(ws, 0, WS_END, 0x00020000);
  const v4u empty4 = {EMPTY, EMPTY, EMPTY, EMPTY};

  if (wv == 0) {
    __builtin_amdgcn_s_setprio(3);
    v2u xr[5];
#pragma unroll
    for (int t = 0; t < 5; ++t) xr[t] = *(const GAS v2u*)(x + (lane >> 5) * D + t * 128 + (lane & 31) * 4);
    v4u rq[20];
    const __amdgpu_buffer_rsrc_t rq_rs = __builtin_amdgcn_make_buffer_rsrc((void*)(wq + (size_t)g8 * 80 * (H * DH) + idx * DH), 0, (79 * H * DH + DH) * 2, 0x00020000);
#pragma unroll
    for (int i = 0; i < 20; ++i) rq[i] = __builtin_amdgcn_raw_buffer_load_b128(rq_rs, (unsigned)(4 * g * (H * DH) + cs * 8) * 2u, ((i >> 2) * 16 + (i & 3)) * (H * DH) * 2, NT_W ? 2 : 0);
    SCHED();
    if (KV_GATE == 2) VMWAIT(24);
    if (KV_GATE > 2) __builtin_amdgcn_s_sleep(KV_GATE);
    flag[F_GO] = 1u;
    STAMP(19);
    TAGS();
    float xv[20];
#pragma unroll
    for (int t = 0; t < 5; ++t) { xv[4 * t] = lo2f(xr[t].x); xv[4 * t + 1] = hi2f(xr[t].x); xv[4 * t + 2] = lo2f(xr[t].y); xv[4 * t + 3] = hi2f(xr[t].y); }
    float sm = 0.f;
#pragma unroll
    for (int i = 0; i < 20; ++i) sm += xv[i];
    const float mean = sumx16(sum16(sm)) * (1.f / D);
    float sq = 0.f;
#pragma unroll
    for (int i = 0; i < 20; ++i) { const float d = xv[i] - mean; sq = fmaf(d, d, sq); }
    const float var = rbf(sumx16(sum16(sq)) * (1.f / D)), mb = rbf(mean);
    const float rden = __builtin_amdgcn_rcpf(rbf(__builtin_amdgcn_sqrtf(rbf(var + 1e-5f))));
#pragma unroll
    for (int t = 0; t < 5; ++t) {
      const unsigned d0 = cvtpk(xv[4 * t] - mb, xv[4 * t + 1] - mb), d1 = cvtpk(xv[4 * t + 2] - mb, xv[4 * t + 3] - mb);
      *(LAS v2u*)(L + L_XN + ((lane >> 5) * D + t * 128 + (lane & 31) * 4) * 2) = v2u{cvtpk(lo2f(d0) * rden, hi2f(d0) * rden), cvtpk(lo2f(d1) * rden, hi2f(d1) * rden)};
    }
    LDS_WAIT();
    STAMP(20);
    v2u af[5];
#pragma unroll
    for (int t = 0; t < 5; ++t) af[t] = *(const LAS v2u*)(L + L_XN + ((lane & 1) * D + g8 * 80 + t * 16 + 4 * g) * 2);
    f32x4 acc[8];
#pragma unroll
    for (int c = 0; c < 8; ++c) acc[c] = f32x4{0.f, 0.f, 0.f, 0.f};
#pragma unroll
    for (int t = 0; t < 5; ++t) {
      v2u bt[8];
      tr4(rq + 4 * t, bt);
#pragma unroll
      for (int c = 0; c < 8; ++c) acc[c] = mfma16(af[t], bt[c], acc[c]);
      SCHED();
    }
    KEEP8(acc);
#pragma unroll
    for (int bb = 0; bb < 2; ++bb) {
      const float v0 = sel4(g, acc[0][bb], acc[2][bb], acc[4][bb], acc[6][bb]), v1 = sel4(g, acc[1][bb], acc[3][bb], acc[5][bb], acc[7][bb]);
      st_sc1(rs, WS_QP + (unsigned)(((idx * 2 + bb) * 8 + g8) * 128) * 8u + (unsigned)lane * 16u, v4u{pack24(v0, tlo), thi, pack24(v1, tlo), thi});
    }
    flag[F_QPUB] = 1u;
    STAMP(1);
    if (t1 >= 0 || is_ffn || is_red) {
      lds_wait_ge(flag + FL_AT, FL_AT == F_Q ? 1u : 8u);
      const unsigned tl = (unsigned)tag, th_ = (unsigned)(tag >> 32);
      _Pragma("nounroll") for (unsigned n = 0; n < SPIN_G; ++n) {
        const v4u f0 = ld_sc1(rs, WS_FL + (unsigned)lane * 16u), f1 = ld_sc1(rs, WS_FL + 1024u + (unsigned)lane * 16u);
        if (__all(((f0.x ^ tl) | (f0.y ^ th_) | (f0.z ^ tl) | (f0.w ^ th_) | (f1.x ^ tl) | (f1.y ^ th_) | (f1.z ^ tl) | (f1.w ^ th_)) == 0u)) break;
        MEMBAR();
        __builtin_amdgcn_s_sleep(4);
      }
      flag[F_ALL] = 1u;
      STAMP(15);
    }
    STAMP_FLUSH();
    return;
  }

  if (wv <= 8) {
    const int kw = wv - 1;
    const size_t kvo = ((size_t)(b * H + h) * SK + (SK - W) + s * 256 + kw * 32) * DH;
    const __amdgpu_buffer_rsrc_t k_rs = __builtin_amdgcn_make_buffer_rsrc((void*)(kc + kvo), 0, 32 * DH * 2, 0x00020000);
    const __amdgpu_buffer_rsrc_t v_rs = __builtin_amdgcn_make_buffer_rsrc((void*)(vc + kvo), 0, 32 * DH * 2, 0x00020000);
    const unsigned kvl = KV_LIN ? (unsigned)lane * 16u : (unsigned)(g * 8 * DH + cs * 8) * 2u;
    constexpr int KVS = KV_LIN ? 1024 : DH * 2;
    v4u rk[8], rv[8];
    if (KV_GATE) lds_wait_ge(flag + F_GO, 1u);
#pragma unroll
    for (int i = 0; i < 8; ++i) { if (i >= WN) VMWAIT(WN - 1); rk[i] = __builtin_amdgcn_raw_buffer_load_b128(k_rs, kvl + i * KVS, 0, NT_KV ? 2 : 0); SCHED(); }
#define V_LOAD(i) do { if (DYN && (i) < SCORE_AT) { if (__builtin_amdgcn_readfirstlane(flag[F_Q]) == 0u) VMWAIT(WN - 1); else VMWAIT(WN2 - 1); } else VMWAIT(((i) >= SCORE_AT ? WN2 : WN) - 1); rv[i] = __builtin_amdgcn_raw_buffer_load_b128(v_rs, kvl + (i) * KVS, 0, NT_KV ? 2 : 0); SCHED(); \
      if ((i) + 1 == WO_AT && lane == 0) lds_inc(L, F_WO); if ((i) + 1 == GU_AT && lane == 0) lds_inc(L, F_GU); } while (0)
#pragma unroll
    for (int i = 0; i < SCORE_AT; ++i) V_LOAD(i);
    if (wv == 1) STAMP(3);
    lds_wait_ge(flag + F_Q, 1u);
    const v4u qf = *(const LAS v4u*)(L + L_Q + cs * 16);
    float sc[8];
#pragma unroll
    for (int i = 0; i < 8; ++i) sc[i] = dot2(rk[i].w, qf.w, dot2(rk[i].z, qf.z, dot2(rk[i].y, qf.y, dot2(rk[i].x, qf.x, 0.f))));
#define TR_STEP(HALF, CTRL, BIT) _Pragma("unroll") for (int i = 0; i < (HALF); ++i) { \
      const float t1_ = sc[i] + dpp<CTRL>(sc[i]), t2_ = sc[i + (HALF)] + dpp<CTRL>(sc[i + (HALF)]); sc[i] = (cs & (BIT)) ? t2_ : t1_; }
    TR_STEP(4, XOR8, 8) TR_STEP(2, XOR7, 4) TR_STEP(1, XOR2, 2)
#undef TR_STEP
    const float sv = rbf(rbf(sc[0] + dpp<XOR1>(sc[0])) * 0.08838834764831845f);
    float m = vmax(sv, dpp<XOR2>(sv)); m = vmax(m, dpp<XOR7>(m)); m = vmax(m, dpp<XOR8>(m));
    m = maxx32(maxx16(m));
    const float e = __builtin_amdgcn_exp2f((sv - m) * 1.4426950408889634f);
    float l = e + dpp<XOR2>(e); l += dpp<XOR7>(l); l += dpp<XOR8>(l);
    l = sumx32(sumx16(l));
    if (lane == 0) { *(LAS f32x2*)(L + L_ML + kw * 8) = f32x2{m, l}; lds_inc(L, F_ML); }
#pragma unroll
    for (int i = SCORE_AT; i < 8; ++i) V_LOAD(i);
#undef V_LOAD
    if (wv == 1) STAMP(4);
    v4u bt[8];
    tr_pair(rv[0], rv[1], bt, 0); tr_pair(rv[2], rv[3], bt, 1);
    lds_wait_ge(flag + F_CLR, 1u);
    lds_wait_ge(flag + F_ML, 8u);
    const f32x2 ml = *(const LAS f32x2*)(L + L_ML + (lane & 7) * 8);
    float M = vmax(ml[0], dpp<XOR1>(ml[0])); M = vmax(M, dpp<XOR2>(M)); M = vmax(M, dpp<XOR7>(M));
    float Ls = ml[1] * __builtin_amdgcn_exp2f((ml[0] - M) * 1.4426950408889634f);
    Ls += dpp<XOR1>(Ls); Ls += dpp<XOR2>(Ls); Ls += dpp<XOR7>(Ls);
    const float pn = e * (__builtin_amdgcn_exp2f((m - M) * 1.4426950408889634f) * __builtin_amdgcn_rcpf(Ls));
    const float ep = __uint_as_float(cvtpk(pn, dpp<XOR2>(pn)));
    v4u pf = {__float_as_uint(dpp<0x150>(ep)), __float_as_uint(dpp<0x154>(ep)), __float_as_uint(dpp<0x158>(ep)), __float_as_uint(dpp<0x15C>(ep))};
    const unsigned lse = __float_as_uint(fmaf(M, 1.4426950408889634f, __builtin_amdgcn_logf(Ls))) >> ((lane & 1) * 16);
    unsigned lb0 = lse & 0xffu, lb1 = (lse >> 8) & 0xffu;
    unsigned o_off = WS_OP + (unsigned)(h * 4 + g) * 1024u + (unsigned)((b * 4 + s) * 128 + cs * 8);
    unsigned w_off = L_OP + kw * 512 + cs * 4, r_off = L_OP + ((cs >> 1) * 16 + 4 * g + 2 * (cs & 1)) * 4;
    asm volatile("" : "+v"(lb0), "+v"(lb1), "+v"(pf), "+v"(o_off), "+v"(w_off), "+v"(r_off));
    SCHED();
    tr_pair(rv[4], rv[5], bt, 2);
    SCHED();
    tr_pair(rv[6], rv[7], bt, 3);
    f32x4 acc[8];
#pragma unroll
    for (int c = 0; c < 8; ++c) acc[c] = mfma32(pf, bt[c], f32x4{0.f, 0.f, 0.f, 0.f});
    KEEP8(acc);
    unsigned old = 0;
    if (lane < 16) {
#pragma unroll
      for (int c = 0; c < 8; ++c) *(LAS float*)(L + w_off + c * 64) = acc[c][0];
      if (lane == 0) old = lds_inc_rtn(L, F_OP);
    }
    if (__builtin_amdgcn_readfirstlane(old) == 7u) {
      MEMBAR();
      const LAS f32x2* pp = (const LAS f32x2*)(L + r_off);
      const f32x2 o = ((pp[0] + pp[64]) + (pp[128] + pp[192])) + ((pp[256] + pp[320]) + (pp[384] + pp[448]));
      st_sc1_64(rs, o_off, v2u{(min(__float_as_uint(o[0]), 0xfffffeffu) & 0xffffff00u) | lb0, (min(__float_as_uint(o[1]), 0xfffffeffu) & 0xffffff00u) | lb1});
      flag[F_OPUB] = 1u;
      STAMP(6);
    }
    if (is_red) {
      const int col = ((rd_ >> 2) & 1) * D + (rd_ >> 3) * 128 + (rd_ & 1) * 64 + (lane & 7) * 8 + ((rd_ >> 1) & 1) * 4 + (lane >> 4);
      const float xv = lo2f(x[col]);
      lds_wait_ge(flag + F_ALL, 1u);
      if (T2_DELAY) __builtin_amdgcn_s_sleep(T2_DELAY);
      float tot; unsigned tpoll = 0;
      if (reduce_inbox<128>(rs, WS_T1 + (unsigned)rd_ * 16384u, kw, lane, L, L_RED, F_R1, tot, tpoll)) {
        const float hv = rbf(xv + rbf(tot));
        const auto sw = __builtin_amdgcn_permlane16_swap(__float_as_uint(hv), __float_as_uint(hv), false, false);
        const unsigned hp = min((sw[0] >> 16) | (sw[1] & 0xffff0000u), EMPTY - 1u);
        const auto sv2 = __builtin_amdgcn_permlane32_swap(hp, hp, false, false);
        if (HREP || lane < 8) st_sc1_64(rs, WS_HB + (unsigned)(lane >> 3) * HB_REP + (unsigned)(rd_ * 64 + (lane & 7) * 8), v2u{sv2[0], sv2[1]});
        *(LAS float*)(L + L_HV + ((lane >> 4) * 8 + (lane & 7)) * 4) = hv;
        if (rd_ == 0) { const u64 e1 = load_epoch(ws); if (lane == 0) *(GAS u64*)(ws + WS_EPOCH) = e1; }
        STAMP(10);
      }
      if (T4_DELAY) __builtin_amdgcn_s_sleep(T4_DELAY);
      if (reduce_inbox<NFFN>(rs, WS_T3 + (unsigned)rd_ * 16384u, kw, lane, L, L_RED + 1024, F_R2, tot, tpoll)) {
        const float hv = *(const LAS float*)(L + L_HV + ((lane >> 4) * 8 + (lane & 7)) * 4);
        if ((lane & 8) == 0) out[col] = (u16)cvtpk(hv + rbf(tot), 0.f);
        STAMP(13);
      }
    } else if (is_ffn && kw < 5) {
      lds_wait_ge(flag + F_OPUB, 1u);
      v4u rdn[8], bd[8];
      const u16* db = wd + (size_t)(f * 32 + g * 8) * D + kw * 128 + cs * 8;
#pragma unroll
      for (int i = 0; i < 8; ++i) rdn[i] = ldg<NT_W>(db + (size_t)i * D);
      SCHED();
      tr8(rdn, bd);
      PIN8(bd);
      const unsigned d_off = tile_off(WS_T3, f, kw, lane);
      f32x2 colsum = {0.f, 0.f};
      int pass = 0;
      asm volatile("" : "+s"(pass));
      _Pragma("nounroll") for (;; ++pass) {
        const LAS unsigned char* Lp = L + pass * 8192;
        const LAS float* gu = (const LAS float*)(Lp + L_GU) + (8 * g + (cs & 7)) * 2 + (cs >> 3);
        BAR();
        f32x2 t[10];
#pragma unroll
        for (int i = 0; i < 10; ++i) t[i] = f32x2{gu[i * 128], gu[i * 128 + 64]};
        const f32x2 st = *(const LAS f32x2*)(L + L_ST + (cs >> 3) * 8);
        SCHED();
        const float mb = st[0], rden = st[1];
        const f32x2 raw = (((t[0] + t[1]) + (t[2] + t[3])) + ((t[4] + t[5]) + (t[6] + t[7]))) + (t[8] + t[9]);
        const unsigned gur = cvtpk(fmaf(-mb, colsum[0], raw[0]) * rden, fmaf(-mb, colsum[1], raw[1]) * rden);
        colsum = raw;
        const float gv = lo2f(gur), uv = hi2f(gur);
        const float act = rbf(gv * __builtin_amdgcn_rcpf(1.f + __builtin_amdgcn_exp2f(gv * -1.4426950408889634f))) * uv;
        const float pk = __uint_as_float(cvtpk(act, dpp<XOR1>(act)));
        const v4u a2 = join_quads(__float_as_uint(dpp<0x00>(pk)), __float_as_uint(dpp<0xAA>(pk)));
        f32x4 acc2[8];
#pragma unroll
        for (int c = 0; c < 8; ++c) acc2[c] = mfma32(a2, bd[c], f32x4{0.f, 0.f, 0.f, 0.f});
        KEEP8(acc2);
        publish_tile(rs, pass ? d_off : WS_END, g, acc2);
        if (pass) break;
      }
      if (wv == 1) STAMP(12);
    } else if (is_ffn && kw == 5) {
      BAR();
      lds_wait_ge(flag + F_ALL, 1u);
      const unsigned off = WS_HB + (unsigned)(HREP ? g8 : 0) * HB_REP + (unsigned)lane * 16u, off2 = lane < 32 ? off + 2048u : WS_END;
      v4u h0, h1, h2;
      _Pragma("nounroll") for (unsigned n = 0; n < SPIN_G; ++n) {
        h0 = ld_sc1(rs, off); h1 = ld_sc1(rs, off + 1024u); h2 = ld_sc1(rs, off2);
        if (!__any(has_empty(h0) || has_empty(h1) || has_empty(h2))) break;
        MEMBAR();
      }
      float s1[3] = {0.f, 0.f, 0.f}, s2[3] = {0.f, 0.f, 0.f};
#pragma unroll
      for (int i = 0; i < 4; ++i) {
        s1[0] = dot2(h0[i], 0x3f803f80u, s1[0]); s2[0] = dot2(h0[i], h0[i], s2[0]);
        s1[1] = dot2(h1[i], 0x3f803f80u, s1[1]); s2[1] = dot2(h1[i], h1[i], s2[1]);
        s1[2] = dot2(h2[i], 0x3f803f80u, s1[2]); s2[2] = dot2(h2[i], h2[i], s2[2]);
      }
      const float mean = sumx32(sum16((s1[0] + s1[1]) + s1[2])) * (1.f / D), msq = sumx32(sum16((s2[0] + s2[1]) + s2[2])) * (1.f / D);
      const float var = rbf(vmax(fmaf(-mean, mean, msq), 0.f));
      const float rden = __builtin_amdgcn_rcpf(rbf(__builtin_amdgcn_sqrtf(rbf(var + 1e-5f))));
      if ((lane & 47) == 0) *(LAS f32x2*)(L + L_ST + g * 8) = f32x2{rbf(mean), rden};
      BAR();
    }
    STAMP_FLUSH();
    return;
  }

  const int ww = wv - 9;
  const unsigned p_off = t1 >= 0 ? tile_off(WS_T1, t1, ww, lane) : tile_off(WS_T3, f, ww, lane);
  TAGS();
  if (ww == 4) {
    lds_wait_ge(flag + F_GO, 1u);
    if (CLR_DELAY) __builtin_amdgcn_s_sleep(CLR_DELAY);
    if (t1 >= 0 || is_ffn) {
#pragma unroll
      for (int i = 0; i < 5; ++i) st_sc1(rs, p_off - (unsigned)i * (8u * 16384u), empty4);
    }
    if (lane < 32) st_sc1(rs, WS_OP + (unsigned)(h * 4 + (lane >> 3)) * 1024u + (unsigned)((b * 4 + s) * 128 + (lane & 7) * 16), empty4);
    if (is_red && lane < 32) st_sc1(rs, WS_HB + (unsigned)(lane >> 2) * HB_REP + (unsigned)(rd_ * 64 + (lane & 3) * 16), empty4);
    VMWAIT(0);
    if (lane == 0) st_sc1_64(rs, WS_FL + (unsigned)w * 8u, v2u{(unsigned)tag, (unsigned)(tag >> 32)});
    flag[F_CLR] = 1u;
    STAMP(17);
  }
  lds_wait_ge(flag + F_QPUB, 1u);
  if (ww < 4) {
    if (Q_DELAY) __builtin_amdgcn_s_sleep(Q_DELAY);
    const unsigned off = WS_QP + (unsigned)(((h * 2 + b) * 8 + ww * 2) * 128) * 8u + (unsigned)lane * 16u;
    v4u qa, qb;
    _Pragma("nounroll") for (unsigned n = 0; n < SPIN_G; ++n) {
      qa = ld_sc1(rs, off); qb = ld_sc1(rs, off + 1024u);
      if (__all((bad24(qa, tlo, thi) | bad24(qb, tlo, thi)) == 0u)) break;
      MEMBAR();
    }
    *(LAS f32x2*)(L + L_QP + (ww * 128 + lane * 2) * 4) = f32x2{unpack24(qa.x) + unpack24(qb.x), unpack24(qa.z) + unpack24(qb.z)};
    unsigned old = 0;
    if (lane == 0) old = lds_inc_rtn(L, F_QC);
    if (__builtin_amdgcn_readfirstlane(old) == 3u) {
      MEMBAR();
      const LAS f32x2* qp = (const LAS f32x2*)(L + L_QP) + lane;
      const f32x2 q = (qp[0] + qp[64]) + (qp[128] + qp[192]);
      *(LAS unsigned*)(L + L_Q + cs * 16 + g * 4) = cvtpk(q[0], q[1]);
      LDS_WAIT();
      flag[F_Q] = 1u;
      STAMP(2);
    }
  }
  if (t1 < 0 && idx >= 8) {
    const u16* pp = ((lane & 4) ? wu : wg) + (size_t)((idx - 8) * 80 + ww * 16 + (lane >> 3)) * F + (lg0 + min(lane & 3, nlg - 1)) * 64;
    lds_wait_ge(flag + F_WO, 8u);
    if (P1_LATE) lds_wait_ge(flag + F_OPUB, 1u);
    const unsigned p0 = *(const GAS unsigned*)pp;
    if (P2_LATE) lds_wait_ge(flag + F_OPUB, 1u);
    const unsigned p1 = *(const GAS unsigned*)(pp + 8 * F);
    asm volatile("" :: "v"(p0), "v"(p1));
    if (wv == 9) STAMP(18);
  }
  if (t1 < 0 && !is_ffn) { STAMP_FLUSH(); return; }

  if (t1 >= 0) {
    if (WO_AT <= 8) lds_wait_ge(flag + F_WO, 8u); else lds_wait_ge(flag + F_OPUB, 1u);
    v4u ro[8], bo[8];
    const u16* ob = wo + (size_t)(t1 * 32 + g * 2) * D + ww * 128 + cs * 8;
#pragma unroll
    for (int i = 0; i < 8; ++i) ro[i] = ldg<NT_W>(ob + (size_t)(8 * (i & 3) + (i >> 2)) * D);
    SCHED();
    tr8(ro, bo);
    PIN8(bo);
    if (wv == 9) STAMP(7);
    lds_wait_ge(flag + F_ALL, 1u);
    lds_wait_ge(flag + F_OPUB, 1u);
    v4u oc;
    {
      const unsigned off = WS_OP + (unsigned)t1 * 1024u + (unsigned)(((cs >> 3) * 4 + (cs & 3)) * 128 + g * 32 + ((cs >> 2) & 1) * 16);
      _Pragma("nounroll") for (unsigned n = 0; n < SPIN_G; ++n) {
        oc = ld_sc1(rs, off);
        if (!__any(has_empty(oc))) break;
        MEMBAR();
      }
    }
    if (wv == 9) STAMP(8);
    const float ls = __uint_as_float(__builtin_amdgcn_perm(__builtin_amdgcn_perm(oc.w, oc.z, 0x0400u), __builtin_amdgcn_perm(oc.y, oc.x, 0x0400u), 0x05040100u));
    float M = vmax(ls, dpp<XOR1>(ls)); M = vmax(M, dpp<XOR2>(M));
    float fs = __builtin_amdgcn_exp2f(ls - M);
    float a0 = __uint_as_float(oc.x) * fs, a1 = __uint_as_float(oc.y) * fs, a2 = __uint_as_float(oc.z) * fs, a3 = __uint_as_float(oc.w) * fs;
    fs += dpp<XOR1>(fs); a0 += dpp<XOR1>(a0); a1 += dpp<XOR1>(a1); a2 += dpp<XOR1>(a2); a3 += dpp<XOR1>(a3);
    fs += dpp<XOR2>(fs); a0 += dpp<XOR2>(a0); a1 += dpp<XOR2>(a1); a2 += dpp<XOR2>(a2); a3 += dpp<XOR2>(a3);
    const float rl = __builtin_amdgcn_rcpf(fs);
    const v4u af = join_quads(cvtpk(a0 * rl, a1 * rl), cvtpk(a2 * rl, a3 * rl));
    f32x4 acc[8];
#pragma unroll
    for (int c = 0; c < 8; ++c) acc[c] = mfma32(af, bo[c], f32x4{0.f, 0.f, 0.f, 0.f});
    KEEP8(acc);
    publish_tile(rs, p_off, g, acc);
    if (wv == 9) STAMP(9);
    STAMP_FLUSH();
    return;
  }

  v4u rg[16], bg[16];
  const u16* gb = ((cs & 4) ? wu : wg) + (size_t)(ww * 128 + g * 16 + (cs >> 3) * 4) * F + f * 32 + (cs & 3) * 8;
  if (GU_AT <= 8) lds_wait_ge(flag + F_GU, 8u); else lds_wait_ge(flag + F_OPUB, 1u);
  if (GU_DELAY) __builtin_amdgcn_s_sleep(GU_DELAY);
#pragma unroll
  for (int i = 0; i < 16; ++i) { if (i >= GW) VMWAIT(GW - 1);  rg[i] = ldg<NT_GU>(gb + (size_t)((i >> 3) * 64 + ((i >> 2) & 1) * 8 + (i & 3)) * F);
    SCHED(); }
  tr8(rg, bg); tr8(rg + 8, bg + 8);
  PIN8(bg); PIN8(bg + 8);
  const unsigned h_off = WS_HB + (unsigned)(HREP ? g8 : 0) * HB_REP + (unsigned)((ww * 8 + (cs & 1) * 4 + (cs >> 3) * 2) * 64 + g * 16);
  int pass = 0;
  asm volatile("" : "+s"(pass));
  _Pragma("nounroll") for (;; ++pass) {
    v4u a0 = {0x3f803f80u, 0x3f803f80u, 0x3f803f80u, 0x3f803f80u}, a1 = a0;
    if (pass) {
      lds_wait_ge(flag + F_ALL, 1u);
      if (wv == 9) STAMP(14);
      _Pragma("nounroll") for (unsigned n = 0; n < SPIN_G; ++n) {
        a0 = ld_sc1(rs, h_off); a1 = ld_sc1(rs, h_off + 64u);
        if (!__any(has_empty(a0) || has_empty(a1))) break;
        MEMBAR();
      }
      if (wv == 9) STAMP(11);
    }
    LAS unsigned char* Lp = L + pass * 8192;
    f32x4 acc[8];
#pragma unroll
    for (int c = 0; c < 8; ++c) acc[c] = mfma32(a1, bg[8 + c], mfma32(a0, bg[c], f32x4{0.f, 0.f, 0.f, 0.f}));
    KEEP8(acc);
    if (g == (cs >> 3) * 2) {
      LAS f32x2* o = (LAS f32x2*)(Lp + L_GU + (ww * 2 + (cs >> 3)) * 512 + (cs & 7) * 64);
#pragma unroll
      for (int c = 0; c < 8; ++c) o[c] = f32x2{acc[c][0], acc[c][1]};
    }
    BAR();
    if (pass) break;
  }
  STAMP_FLUSH();
}

extern "C" void kernel_launch(void* const* d_in, const int* in_sizes, int n_in, void* d_out, int out_size, void* d_ws, size_t ws_size, hipStream_t stream) {
  fused_block<<<NWG, NTHR, 0, stream>>>((const u16*)d_in[0], (const u16*)d_in[1], (const u16*)d_in[2], (const u16*)d_in[3], (const u16*)d_in[4],
                                        (const u16*)d_in[6], (const u16*)d_in[7], (const u16*)d_in[8], (u16*)d_out, (unsigned char*)d_ws);
}
```

```cpp
#include <hip/hip_runtime.h>
#include <hip/hip_bf16.h>

typedef unsigned short u16;
typedef unsigned long long u64;
typedef unsigned v4u __attribute__((ext_vector_type(4)));
typedef unsigned v2u __attribute__((ext_vector_type(2)));
typedef float f32x4 __attribute__((ext_vector_type(4)));
typedef float f32x2 __attribute__((ext_vector_type(2)));
typedef __bf16 bf16x2 __attribute__((ext_vector_type(2)));
typedef __bf16 bf16x8 __attribute__((ext_vector_type(8)));
typedef short s16x4 __attribute__((ext_vector_type(4)));
typedef __amdgpu_buffer_rsrc_t rsrc_t;
#define GAS __attribute__((address_space(1)))
#define LAS __attribute__((address_space(3)))

constexpr int D = 640, H = 32, DH = 128, SK = 4096, W = 1024, F = 1728;
constexpr int NWG = 256, NWAVE = 14, NTHR = NWAVE * 64;
constexpr int NFFN = 54;
constexpr int NRED = 40;
constexpr unsigned SPIN_G = 200000, SPIN_L = 4000000;
constexpr unsigned EMPTY = 0xffffffffu;
constexpr float LOG2E = 1.4426950408889634f;
constexpr int PER16 = 466, ROFF = 56, CAP = 5, SCORE_AT = 5, FL_AT = 480;
constexpr int PF0 = 85, PFS = 13, GLAG = 82, GSTEP = 4, DPF = 50, DPS = 6, DN0 = 40, DNS = 2;
constexpr int Q_DELAY = 20, O_DELAY = 11, T2_DELAY = 6, T4_DELAY = 32;
constexpr int CODE_BYTES = 19200;

constexpr unsigned WS_EPOCH = 0;
constexpr unsigned WS_FL = 4096;
constexpr unsigned SIDE = 8192;
constexpr unsigned WS_HB = 16384;
constexpr unsigned WS_OP = WS_HB + 2 * 16384;
constexpr unsigned WS_T1 = WS_OP + 8 * 16384;
constexpr unsigned WS_T3 = WS_T1 + 20 * 32768;
constexpr unsigned WS_PR = WS_T3 + 20 * 16384;
constexpr unsigned WS_QP = WS_PR + 4 * 16384;
constexpr unsigned WS_END = WS_QP + H * 2 * 8 * 128 * 8;

enum { F_Q = 0, F_VI, F_ML, F_OP, F_OPUB, F_ALL, F_T1, F_QPUB, F_QC, F_CS, F_TAG, F_TAGLO, F_TAGHI };
constexpr int L_FLAG = 0;
constexpr int L_XN = 64;
constexpr int L_Q = L_XN + 2560;
constexpr int L_ML = L_Q + 256;
constexpr int L_OP = L_ML + 64;
constexpr int L_ST = L_OP + 4096;
constexpr int L_GU = L_ST + 64;
constexpr int L_CS = L_GU + 8192;
constexpr int L_QP = L_GU;
constexpr int L_RED = L_GU;
constexpr int LDS_BYTES = L_CS + 5120;

constexpr int SC1 = 16, NT = 2;
#define MEMBAR() asm volatile("" ::: "memory")
#define LDS_WAIT() asm volatile("s_waitcnt lgkmcnt(0)" ::: "memory")
#define VMWAIT(n) asm volatile("s_waitcnt vmcnt(%0)" :: "n"(n) : "memory")
#define SCHED() __builtin_amdgcn_sched_barrier(0)
#define BAR() do { LDS_WAIT(); __builtin_amdgcn_s_barrier(); MEMBAR(); } while (0)
#define PIN4(a) asm volatile("" : "+v"((a)[0]), "+v"((a)[1]), "+v"((a)[2]), "+v"((a)[3]))
#define PIN8(a) do { PIN4(a); PIN4((a) + 4); } while (0)
#define KEEP8(a) asm volatile("" :: "v"((a)[0]), "v"((a)[1]), "v"((a)[2]), "v"((a)[3]), "v"((a)[4]), "v"((a)[5]), "v"((a)[6]), "v"((a)[7]))
#define POLL _Pragma("nounroll") for (unsigned n_ = 0; n_ < SPIN_G; ++n_)

__device__ __forceinline__ rsrc_t make_rsrc(const void* p, unsigned bytes) { return __builtin_amdgcn_make_buffer_rsrc((void*)p, 0, bytes, 0x00020000); }
__device__ __forceinline__ v4u ld_nt(const u16* p) { return __builtin_nontemporal_load((const GAS v4u*)p); }
__device__ __forceinline__ v4u ld_sc1(rsrc_t rs, unsigned off) { return __builtin_amdgcn_raw_buffer_load_b128(rs, off, 0, SC1); }
__device__ __forceinline__ void st_sc1(rsrc_t rs, unsigned off, v4u v) { __builtin_amdgcn_raw_buffer_store_b128(v, rs, off, 0, SC1); }
__device__ __forceinline__ void st_sc1(rsrc_t rs, unsigned off, v2u v) { __builtin_amdgcn_raw_buffer_store_b64(v, rs, off, 0, SC1); }
__device__ __forceinline__ unsigned cvtpk(float lo, float hi) {
  asm("" : "+v"(lo));
  asm("" : "+v"(hi));
  f32x2 v = {lo, hi};
  return __builtin_bit_cast(unsigned, __builtin_convertvector(v, bf16x2));
}
__device__ __forceinline__ float lo2f(unsigned u) { return __uint_as_float(u << 16); }
__device__ __forceinline__ float hi2f(unsigned u) { return __uint_as_float(u & 0xffff0000u); }
__device__ __forceinline__ float rbf(float f) { return lo2f(cvtpk(f, f)); }
__device__ __forceinline__ float vmax(float a, float b) { float r; asm("v_max_f32 %0, %1, %2" : "=v"(r) : "v"(a), "v"(b)); return r; }
__device__ __forceinline__ float ex2(float x) { return __builtin_amdgcn_exp2f(x); }
__device__ __forceinline__ float rcp(float x) { return __builtin_amdgcn_rcpf(x); }
__device__ __forceinline__ float ln_rden(float var) { return rcp(rbf(__builtin_amdgcn_sqrtf(rbf(rbf(var) + 1e-5f)))); }
__device__ __forceinline__ float dot2(unsigned a, unsigned b, float c) {
  return __builtin_amdgcn_fdot2_f32_bf16(__builtin_bit_cast(bf16x2, a), __builtin_bit_cast(bf16x2, b), c, false);
}
template <int CTRL> __device__ __forceinline__ unsigned dppu(unsigned x) { return (unsigned)__builtin_amdgcn_mov_dpp((int)x, CTRL, 0xf, 0xf, true); }
template <int CTRL> __device__ __forceinline__ float dpp(float x) { return __uint_as_float(dppu<CTRL>(__float_as_uint(x))); }
constexpr int XOR1 = 0xB1, XOR2 = 0x4E, XOR7 = 0x141, XOR8 = 0x128;
__device__ __forceinline__ float sum16(float x) { x += dpp<XOR1>(x); x += dpp<XOR2>(x); x += dpp<XOR7>(x); x += dpp<XOR8>(x); return x; }
__device__ __forceinline__ f32x2 swap16(float a, float b) {
  const auto s = __builtin_amdgcn_permlane16_swap(__float_as_uint(a), __float_as_uint(b), false, false);
  return f32x2{__uint_as_float(s[0]), __uint_as_float(s[1])};
}
__device__ __forceinline__ f32x2 swap32(float a, float b) {
  const auto s = __builtin_amdgcn_permlane32_swap(__float_as_uint(a), __float_as_uint(b), false, false);
  return f32x2{__uint_as_float(s[0]), __uint_as_float(s[1])};
}
__device__ __forceinline__ float sumx16(float x) { const f32x2 s = swap16(x, x); return s[0] + s[1]; }
__device__ __forceinline__ float sumx32(float x) { const f32x2 s = swap32(x, x); return s[0] + s[1]; }
__device__ __forceinline__ float maxx16(float x) { const f32x2 s = swap16(x, x); return vmax(s[0], s[1]); }
__device__ __forceinline__ float maxx32(float x) { const f32x2 s = swap32(x, x); return vmax(s[0], s[1]); }
__device__ __forceinline__ f32x4 mfma32(v4u a, v4u b, f32x4 c) {
  return __builtin_amdgcn_mfma_f32_16x16x32_bf16(__builtin_bit_cast(bf16x8, a), __builtin_bit_cast(bf16x8, b), c, 0, 0, 0);
}
__device__ __forceinline__ f32x4 mfma16(v2u a, v2u b, f32x4 c) {
  return __builtin_amdgcn_mfma_f32_16x16x16bf16_1k(__builtin_bit_cast(s16x4, a), __builtin_bit_cast(s16x4, b), c, 0, 0, 0);
}
template <typename V> __device__ __forceinline__ void tr_pair(const v4u& r0, const v4u& r1, V* t, int pr) {
#pragma unroll
  for (int dw = 0; dw < 4; ++dw) {
    t[2 * dw][pr] = __builtin_amdgcn_perm(r1[dw], r0[dw], 0x05040100u);
    t[2 * dw + 1][pr] = __builtin_amdgcn_perm(r1[dw], r0[dw], 0x07060302u);
  }
}
__device__ __forceinline__ void tr8(const v4u* r, v4u* t) {
#pragma unroll
  for (int pr = 0; pr < 4; ++pr) tr_pair(r[2 * pr], r[2 * pr + 1], t, pr);
}
__device__ __forceinline__ float sel4(int g, float a, float b, float c, float d) {
  const float x = (g & 1) ? b : a, y = (g & 1) ? d : c;
  return (g & 2) ? y : x;
}
__device__ __forceinline__ v4u join_quads(unsigned a, unsigned b) {
  return v4u{(unsigned)__builtin_amdgcn_update_dpp((int)a, (int)a, XOR7, 0xf, 0xa, false), (unsigned)__builtin_amdgcn_update_dpp((int)b, (int)b, XOR7, 0xf, 0xa, false),
             (unsigned)__builtin_amdgcn_update_dpp((int)a, (int)a, XOR7, 0xf, 0x5, false), (unsigned)__builtin_amdgcn_update_dpp((int)b, (int)b, XOR7, 0xf, 0x5, false)};
}
__device__ __forceinline__ unsigned gpack(float v, unsigned tlo) { return ((__float_as_uint(v) + 0x8u) & 0xfffffff0u) | tlo; }
__device__ __forceinline__ float gval(unsigned u) { return __uint_as_float(u & 0xfffffff0u); }
__device__ __forceinline__ unsigned gbad(v4u g, unsigned tlo, unsigned thi) { return (((g.x ^ tlo) | (g.z ^ tlo)) & 0xfu) | (g.y ^ thi) | (g.w ^ thi); }
__device__ __forceinline__ unsigned top(v4u a, unsigned m) { return max(max(max(a.x, a.y), a.z), max(a.w, m)); }
__device__ __forceinline__ unsigned enc(float v) { return min(__float_as_uint(v), EMPTY - 1u); }

__device__ __forceinline__ void lds_wait_ge(volatile LAS unsigned* f, unsigned want) {
  for (unsigned n = 0; *f < want && n < SPIN_L; ++n) __builtin_amdgcn_s_sleep(1);
  MEMBAR();
}
__device__ __forceinline__ void lds_inc(LAS unsigned char* L, int i) {
  asm volatile("ds_add_u32 %0, %1" :: "v"((unsigned)(size_t)(L + L_FLAG + 4 * i)), "v"(1u) : "memory");
}
__device__ __forceinline__ unsigned lds_ticket(LAS unsigned char* L, int i, int lane) {
  unsigned r = 0;
  if (lane == 0) asm volatile("ds_add_rtn_u32 %0, %1, %2\n\ts_waitcnt lgkmcnt(0)" : "=v"(r) : "v"((unsigned)(size_t)(L + L_FLAG + 4 * i)), "v"(1u) : "memory");
  r = __builtin_amdgcn_readfirstlane(r);
  MEMBAR();
  return r;
}
__device__ __forceinline__ u64 load_epoch(const unsigned char* ws) {
  u64 e;
  asm volatile("s_load_dwordx2 %0, %1, 0x0\n\ts_waitcnt lgkmcnt(0)" : "=s"(e) : "s"(ws) : "memory");
  return e + 1ull;
}
#define TAGS() const u64 tag = load_epoch(ws) * 0x9E3779B97F4A7C15ull; SCHED(); \
  unsigned thi = (unsigned)tag, tlo = (unsigned)(tag >> 32) & 0xfu; asm volatile("" : "+s"(thi), "+s"(tlo))
__device__ __forceinline__ unsigned now32() { u64 t; asm volatile("s_memrealtime %0\n\ts_waitcnt lgkmcnt(0)" : "=s"(t) :: "memory"); return (unsigned)t; }
__device__ __forceinline__ void wait_until(unsigned t) { while ((int)(now32() - t) < 0) __builtin_amdgcn_s_sleep(1); }

template <bool T1> __device__ __forceinline__ unsigned inbox_off(int r) { return (T1 ? WS_T1 : WS_T3) + (unsigned)((r >> 3) * 4 + (r & 3)) * (T1 ? 32768u : 16384u) + (unsigned)((r >> 2) & 1) * SIDE; }
template <bool T1> __device__ __forceinline__ unsigned tile_off(int prod, int tile, int lane) {
  return inbox_off<T1>(tile * 8 + (lane >> 3)) + (unsigned)((prod >> 6) * 16384 + (prod & 63) * 128 + (lane & 7) * 16);
}
__device__ __forceinline__ unsigned o_slot(int h, int q) { return WS_OP + (unsigned)((h >> 3) * 2 + ((h >> 1) & 1)) * 16384u + (unsigned)((h >> 2) & 1) * SIDE + (unsigned)((h & 1) * 4 + q) * 1024u; }
__device__ __forceinline__ unsigned h_copy(int c) { return WS_HB + (unsigned)((c >> 1) & 1) * 16384u + (unsigned)(c >> 2) * SIDE + (unsigned)(c & 1) * 4096u; }
__device__ __forceinline__ unsigned side_mask(volatile LAS unsigned* flag_all) {
  lds_wait_ge(flag_all, 1u);
  return (__builtin_amdgcn_readfirstlane(*flag_all) & 2u) ? SIDE : 0u;
}
__device__ __forceinline__ int gu_row(int i) { return (i >> 3) * 64 + ((i >> 2) & 1) * 8 + (i & 3); }
__device__ __forceinline__ unsigned time_line(const unsigned char* p) {
  u64 t0, t1; unsigned v;
  asm volatile("s_memtime %0\n\ts_waitcnt lgkmcnt(0)\n\ts_load_dword %2, %3, 0x0\n\ts_waitcnt lgkmcnt(0)\n\ts_memtime %1\n\ts_waitcnt lgkmcnt(0)" : "=&s"(t0), "=&s"(t1), "=&s"(v) : "s"(p) : "memory");
  return (unsigned)(t1 - t0);
}
__device__ __forceinline__ void tile_product(rsrc_t rs, unsigned off, int g, v4u a, const v4u* bt) {
  f32x4 acc[8];
#pragma unroll
  for (int c = 0; c < 8; ++c) acc[c] = mfma32(a, bt[c], f32x4{0.f, 0.f, 0.f, 0.f});
  KEEP8(acc);
  const bool up = g & 1;
  st_sc1(rs, off, v4u{enc(up ? acc[4][0] : acc[0][0]), enc(up ? acc[5][0] : acc[1][0]), enc(up ? acc[6][0] : acc[2][0]), enc(up ? acc[7][0] : acc[3][0])});
}
template <int NP> __device__ __forceinline__ float reduce_inbox(rsrc_t rs, unsigned base, int rw, int lane, LAS unsigned char* L, int red_off) {
  constexpr int NW = 8;
  constexpr bool TWO = NP > 64;
  const unsigned off = (TWO || rw * 8 + (lane >> 3) < NP) ? base + (unsigned)(TWO ? (rw >> 2) * 16384 + (rw & 3) * 2048 : rw * 1024) + (unsigned)lane * 16u : WS_END;
  const int slot = (lane >> 4) * 8 + (lane & 7);
  unsigned wa = red_off + (rw * 32 + slot) * 4, ra = red_off + (((lane >> 3) & 1) * (NW * 16) + slot) * 4;
  asm volatile("" : "+v"(wa), "+v"(ra));
  v4u a, b = {0u, 0u, 0u, 0u};
  POLL {
    a = ld_sc1(rs, off);
    if (TWO) b = ld_sc1(rs, off + 1024u);
    if (!__any((TWO ? top(b, top(a, 0u)) : top(a, 0u)) == EMPTY)) break;
    MEMBAR();
  }
  float v0 = __uint_as_float(a.x), v1 = __uint_as_float(a.y), v2 = __uint_as_float(a.z), v3 = __uint_as_float(a.w);
  if (TWO) { v0 += __uint_as_float(b.x); v1 += __uint_as_float(b.y); v2 += __uint_as_float(b.z); v3 += __uint_as_float(b.w); }
  const f32x2 s0 = swap32(v0, v2), s1 = swap32(v1, v3);
  const f32x2 s2 = swap16(s0[0] + s0[1], s1[0] + s1[1]);
  float t = s2[0] + s2[1];
  t += dpp<XOR8>(t);
  *(LAS float*)(L + wa) = t;
  BAR();
  const LAS float* rp = (const LAS float*)(L + ra);
  float p[NW / 2];
#pragma unroll
  for (int i = 0; i < NW / 2; ++i) p[i] = rp[i * 32];
#pragma unroll
  for (int st = 1; st < NW / 2; st *= 2)
#pragma unroll
    for (int i = 0; i < NW / 2; i += 2 * st) { p[i] += p[i + st]; asm("" : "+v"(p[i])); }
  return p[0] + dpp<XOR8>(p[0]);
}

__global__ void __launch_bounds__(NTHR, 4) fused_block(const u16* __restrict__ x, const u16* __restrict__ kc, const u16* __restrict__ vc, const u16* __restrict__ wq,
                                                       const u16* __restrict__ wo, const u16* __restrict__ wg, const u16* __restrict__ wu, const u16* __restrict__ wd,
                                                       u16* __restrict__ out, unsigned char* ws_raw) {
  __shared__ __attribute__((aligned(16))) unsigned char lds_raw[LDS_BYTES];
  LAS unsigned char* L = (LAS unsigned char*)lds_raw;
  volatile LAS unsigned* flag = (volatile LAS unsigned*)(L + L_FLAG);
  const int tid = threadIdx.x, lane = tid & 63, wv = __builtin_amdgcn_readfirstlane(tid >> 6), w = blockIdx.x;
  const int g = lane >> 4, cs = lane & 15;
  const int g8 = w & 7, idx = w >> 3, h = g8 + 8 * (idx & 3), b = (idx >> 2) & 1, s = idx >> 3;
  const int t1 = h * 4 + ((idx >> 2) & 3);
  const int rd = w - 128;
  const int lg0 = (g8 * 27) >> 3, nlg = (((g8 + 1) * 27) >> 3) - lg0;
  const int f = 2 * lg0 + idx;
  const bool is_t1 = w >= 128, is_ffn = idx < 2 * nlg, is_red = is_t1 && rd < NRED;
  unsigned cpf[3] = {0u, 0u, 0u};
  if (wv == 9) {
    u64 pc;
    asm volatile("s_getpc_b64 %0" : "=s"(pc));
    const GAS unsigned char* code = (const GAS unsigned char*)(pc & ~255ull);
#pragma unroll
    for (int i = 0; i * 8192 < CODE_BYTES; ++i) cpf[i] = *(const GAS unsigned*)(code + min(i * 8192 + lane * 128, CODE_BYTES - 128));
  }
  if (tid < 16) flag[tid] = 0u;
  asm volatile("" :: "s"(x), "s"(kc), "s"(vc), "s"(wq), "s"(wo), "s"(wg), "s"(wu), "s"(wd), "s"(out), "s"(ws_raw));
  unsigned char* ws = (unsigned char*)(((size_t)ws_raw + 16383) & ~(size_t)16383);
  const rsrc_t rs = make_rsrc(ws, WS_END);

  if (wv == 0) {
    __builtin_amdgcn_s_setprio(3);
    v2u xr[5];
#pragma unroll
    for (int t = 0; t < 5; ++t) xr[t] = *(const GAS v2u*)(x + (lane >> 5) * D + t * 128 + (lane & 31) * 4);
    v4u rq[20];
    const rsrc_t rq_rs = make_rsrc(wq + (size_t)g8 * 80 * (H * DH) + idx * DH, (79 * H * DH + DH) * 2);
#pragma unroll
    for (int i = 0; i < 20; ++i) rq[i] = __builtin_amdgcn_raw_buffer_load_b128(rq_rs, (unsigned)(4 * g * (H * DH) + cs * 8) * 2u, ((i >> 2) * 16 + (i & 3)) * (H * DH) * 2, NT);
    SCHED();
    BAR();
    const unsigned t0 = now32();
    SCHED();
    float xv[20];
#pragma unroll
    for (int t = 0; t < 5; ++t) { xv[4 * t] = lo2f(xr[t].x); xv[4 * t + 1] = hi2f(xr[t].x); xv[4 * t + 2] = lo2f(xr[t].y); xv[4 * t + 3] = hi2f(xr[t].y); }
    float sm = 0.f;
#pragma unroll
    for (int i = 0; i < 20; ++i) sm += xv[i];
    const float mean = sumx16(sum16(sm)) * (1.f / D);
    float sq = 0.f;
#pragma unroll
    for (int i = 0; i < 20; ++i) { const float d = xv[i] - mean; sq = fmaf(d, d, sq); }
    const float rden = ln_rden(sumx16(sum16(sq)) * (1.f / D)), mb = rbf(mean);
#pragma unroll
    for (int t = 0; t < 5; ++t) {
      const unsigned d0 = cvtpk(xv[4 * t] - mb, xv[4 * t + 1] - mb), d1 = cvtpk(xv[4 * t + 2] - mb, xv[4 * t + 3] - mb);
      *(LAS v2u*)(L + L_XN + ((lane >> 5) * D + t * 128 + (lane & 31) * 4) * 2) = v2u{cvtpk(lo2f(d0) * rden, hi2f(d0) * rden), cvtpk(lo2f(d1) * rden, hi2f(d1) * rden)};
    }
    LDS_WAIT();
    v2u af[5];
#pragma unroll
    for (int t = 0; t < 5; ++t) af[t] = *(const LAS v2u*)(L + L_XN + ((lane & 1) * D + g8 * 80 + t * 16 + 4 * g) * 2);
    f32x4 acc[8];
#pragma unroll
    for (int c = 0; c < 8; ++c) acc[c] = f32x4{0.f, 0.f, 0.f, 0.f};
#pragma unroll
    for (int t = 0; t < 5; ++t) {
      v2u bt[8];
      tr_pair(rq[4 * t], rq[4 * t + 1], bt, 0); tr_pair(rq[4 * t + 2], rq[4 * t + 3], bt, 1);
#pragma unroll
      for (int c = 0; c < 8; ++c) acc[c] = mfma16(af[t], bt[c], acc[c]);
      SCHED();
    }
    KEEP8(acc);
    lds_wait_ge(flag + F_TAG, 1u);
    const unsigned thi = flag[F_TAGLO], th = flag[F_TAGHI], tlo = th & 0xfu;
#pragma unroll
    for (int bb = 0; bb < 2; ++bb) {
      const float v0 = sel4(g, acc[0][bb], acc[2][bb], acc[4][bb], acc[6][bb]), v1 = sel4(g, acc[1][bb], acc[3][bb], acc[5][bb], acc[7][bb]);
      st_sc1(rs, WS_QP + (unsigned)(((idx * 2 + bb) * 8 + g8) * 128) * 8u + (unsigned)lane * 16u, v4u{gpack(v0, tlo), thi, gpack(v1, tlo), thi});
    }
    flag[F_QPUB] = 1u;
    {
      wait_until(t0 + FL_AT);
      v4u f0, f1;
      POLL {
        f0 = ld_sc1(rs, WS_FL + (unsigned)lane * 16u); f1 = ld_sc1(rs, WS_FL + 1024u + (unsigned)lane * 16u);
        if (__all(((((f0.x ^ thi) | (f0.z ^ thi) | (f1.x ^ thi) | (f1.z ^ thi)) >> 1) | (f0.y ^ th) | (f0.w ^ th) | (f1.y ^ th) | (f1.w ^ th)) == 0u)) break;
        MEMBAR();
        __builtin_amdgcn_s_sleep(8);
      }
      const int votes = __popcll(__ballot(f0.x & 1u)) + __popcll(__ballot(f0.z & 1u)) + __popcll(__ballot(f1.x & 1u)) + __popcll(__ballot(f1.z & 1u));
      flag[F_ALL] = votes > NWG / 2 ? 3u : 1u;
    }
  } else if (wv <= 8) {
    const int kw = wv - 1;
    const size_t kvo = ((size_t)(b * H + h) * SK + (SK - W) + s * 256) * DH;
    const rsrc_t k_rs = make_rsrc(kc + kvo, 256 * DH * 2), v_rs = make_rsrc(vc + kvo, 256 * DH * 2);
    const unsigned kvl = (unsigned)lane * 16u;
    v4u rk[8], rv[8];
    const unsigned hsh = ((unsigned)w * 0x9E3779B1u) >> 20;
#define KVOFF(i) (((((i) * 8 + kw) * ((hsh & 62) | 1) + (hsh >> 6)) & 63) * 1024)
    asm volatile("" :: "s"(k_rs), "v"(kvl));
    BAR();
    const unsigned tb = now32() + ROFF + ((kw * PER16) >> 7);
#define K_PACE(i) do { if ((i) >= CAP) { VMWAIT(CAP - 1); } wait_until(tb + (((i) * PER16) >> 4)); } while (0)
#define V_PACE(i) do { if (__builtin_amdgcn_readfirstlane(flag[F_Q]) == 0u) VMWAIT(CAP - 1); wait_until(tb + (((8 + (i)) * PER16) >> 4)); } while (0)
#define V_LOAD(i) do { V_PACE(i); rv[i] = __builtin_amdgcn_raw_buffer_load_b128(v_rs, kvl, KVOFF(i), NT); SCHED(); } while (0)
#pragma unroll
    for (int i = 0; i < 8; ++i) { K_PACE(i); rk[i] = __builtin_amdgcn_raw_buffer_load_b128(k_rs, kvl, KVOFF(i), NT); SCHED(); }
    float m, e;
    auto scores = [&]() {
      const v4u qf = *(const LAS v4u*)(L + L_Q + cs * 16);
      float sc[8];
#pragma unroll
      for (int i = 0; i < 8; ++i) sc[i] = dot2(rk[i].w, qf.w, dot2(rk[i].z, qf.z, dot2(rk[i].y, qf.y, dot2(rk[i].x, qf.x, 0.f))));
#define TR_STEP(HALF, CTRL, BIT) _Pragma("unroll") for (int i = 0; i < (HALF); ++i) { \
        const float ta = sc[i] + dpp<CTRL>(sc[i]), tb_ = sc[i + (HALF)] + dpp<CTRL>(sc[i + (HALF)]); sc[i] = (cs & (BIT)) ? tb_ : ta; }
      TR_STEP(4, XOR8, 8) TR_STEP(2, XOR7, 4) TR_STEP(1, XOR2, 2)
      const float sv = rbf(rbf(sc[0] + dpp<XOR1>(sc[0])) * 0.08838834764831845f);
      m = vmax(sv, dpp<XOR2>(sv)); m = vmax(m, dpp<XOR7>(m)); m = vmax(m, dpp<XOR8>(m));
      m = maxx32(maxx16(m));
      e = ex2((sv - m) * LOG2E);
      float l = e + dpp<XOR2>(e); l += dpp<XOR7>(l); l += dpp<XOR8>(l);
      l = sumx32(sumx16(l));
      if (lane == 0) { *(LAS f32x2*)(L + L_ML + kw * 8) = f32x2{m, l}; lds_inc(L, F_ML); }
    };
#pragma unroll
    for (int i = 0; i < SCORE_AT; ++i) V_LOAD(i);
    const bool early = __builtin_amdgcn_readfirstlane(flag[F_Q]) != 0u;
    MEMBAR();
    if (early) scores();
#pragma unroll
    for (int i = SCORE_AT; i < 8; ++i) V_LOAD(i);
    if (lane == 0) lds_inc(L, F_VI);
    if (!early) { lds_wait_ge(flag + F_Q, 1u); scores(); }
    v4u bt[8];
    tr_pair(rv[0], rv[1], bt, 0); tr_pair(rv[2], rv[3], bt, 1);
    lds_wait_ge(flag + F_ML, 8u);
    const f32x2 ml = *(const LAS f32x2*)(L + L_ML + (lane & 7) * 8);
    float M = vmax(ml[0], dpp<XOR1>(ml[0])); M = vmax(M, dpp<XOR2>(M)); M = vmax(M, dpp<XOR7>(M));
    float Ls = ml[1] * ex2((ml[0] - M) * LOG2E);
    Ls += dpp<XOR1>(Ls); Ls += dpp<XOR2>(Ls); Ls += dpp<XOR7>(Ls);
    const float pn = e * (ex2((m - M) * LOG2E) * rcp(Ls));
    const unsigned ep = cvtpk(pn, dpp<XOR2>(pn));
    v4u pf = {dppu<0x150>(ep), dppu<0x154>(ep), dppu<0x158>(ep), dppu<0x15C>(ep)};
    const unsigned lse = ((__float_as_uint(M) & 0xffff0000u) | min((unsigned)fmaf(__builtin_amdgcn_logf(Ls), 8191.f, 0.5f), 0xffffu)) >> ((lane & 1) * 16);
    unsigned lb0 = lse & 0xffu, lb1 = (lse >> 8) & 0xffu;
    unsigned o_off = o_slot(h, g) + (unsigned)((b * 4 + s) * 128 + cs * 8);
    unsigned w_off = L_OP + kw * 512 + cs * 4, r_off = L_OP + ((cs >> 1) * 16 + 4 * g + 2 * (cs & 1)) * 4;
    asm volatile("" : "+v"(lb0), "+v"(lb1), "+v"(pf), "+v"(o_off), "+v"(w_off), "+v"(r_off));
    SCHED();
    tr_pair(rv[4], rv[5], bt, 2);
    SCHED();
    tr_pair(rv[6], rv[7], bt, 3);
    f32x4 acc[8];
#pragma unroll
    for (int c = 0; c < 8; ++c) acc[c] = mfma32(pf, bt[c], f32x4{0.f, 0.f, 0.f, 0.f});
    KEEP8(acc);
    if (lane < 16) {
#pragma unroll
      for (int c = 0; c < 8; ++c) *(LAS float*)(L + w_off + c * 64) = acc[c][0];
    }
    if (lds_ticket(L, F_OP, lane) == 7u) {
      unsigned fa = flag[F_ALL];
      const LAS f32x2* pp = (const LAS f32x2*)(L + r_off);
      const f32x2 o = ((pp[0] + pp[64]) + (pp[128] + pp[192])) + ((pp[256] + pp[320]) + (pp[384] + pp[448]));
      if (__builtin_amdgcn_readfirstlane(fa) == 0u) { lds_wait_ge(flag + F_ALL, 1u); fa = flag[F_ALL]; }
      st_sc1(rs, o_off ^ ((fa & 2u) * (SIDE / 2)), v2u{(min(__float_as_uint(o[0]), 0xfffffeffu) & 0xffffff00u) | lb0, (min(__float_as_uint(o[1]), 0xfffffeffu) & 0xffffff00u) | lb1});
      flag[F_OPUB] = 1u;
    }
    if (is_ffn && kw < 5) {
      lds_wait_ge(flag + F_OPUB, 1u);
      wait_until(now32() + DN0 + kw * DNS);
      v4u rdn[8], bd[8];
      const u16* db = wd + (size_t)(f * 32 + g * 8) * D + kw * 128 + cs * 8;
#pragma unroll
      for (int i = 0; i < 8; ++i) rdn[i] = ld_nt(db + (size_t)i * D);
      SCHED();
      tr8(rdn, bd);
      PIN8(bd);
      unsigned d_off = tile_off<false>(f, kw, lane) ^ side_mask(flag + F_ALL);
      unsigned gr_off = L_GU + ((g >> 1) * 32 + (cs & 7) * 4 + (g & 1) * 2 + (cs >> 3)) * 4, st_off = L_ST + (cs >> 3) * 8;
      asm volatile("" : "+v"(gr_off), "+v"(st_off), "+v"(d_off));
      const LAS float* gu = (const LAS float*)(L + gr_off);
      lds_wait_ge(flag + F_CS, 5u);
      f32x2 colsum = {0.f, 0.f};
#pragma unroll
      for (int i = 0; i < 10; ++i) colsum += f32x2{gu[2048 + i * 128 - (cs >> 3)], gu[2048 + i * 128 + 64 - (cs >> 3)]};
      asm volatile("" : "+v"(colsum));
      BAR();
      f32x2 t[10];
#pragma unroll
      for (int i = 0; i < 10; ++i) t[i] = f32x2{gu[i * 128], gu[i * 128 + 64]};
      const f32x2 st = *(const LAS f32x2*)(L + st_off);
      SCHED();
      const f32x2 raw = (((t[0] + t[1]) + (t[2] + t[3])) + ((t[4] + t[5]) + (t[6] + t[7]))) + (t[8] + t[9]);
      const unsigned gur = cvtpk(fmaf(-st[0], colsum[0], raw[0]) * st[1], fmaf(-st[0], colsum[1], raw[1]) * st[1]);
      const float gv = lo2f(gur), uv = hi2f(gur);
      const float act = rbf(gv * rcp(1.f + ex2(gv * -LOG2E))) * uv;
      const unsigned pk = cvtpk(act, dpp<XOR1>(act));
      tile_product(rs, d_off, g, join_quads(dppu<0x00>(pk), dppu<0xAA>(pk)), bd);
    } else if (is_ffn && kw == 6) {
      lds_wait_ge(flag + F_CS, 1u);
      const unsigned off = (h_copy(g8) ^ side_mask(flag + F_ALL)) + (unsigned)lane * 16u, off2 = lane < 32 ? off + 2048u : WS_END;
      v4u h0, h1, h2;
      POLL {
        h0 = ld_sc1(rs, off); h1 = ld_sc1(rs, off + 1024u); h2 = ld_sc1(rs, off2);
        if (!__any(top(h2, top(h1, top(h0, 0u))) == EMPTY)) break;
        MEMBAR();
      }
      float s1[3] = {0.f, 0.f, 0.f}, s2[3] = {0.f, 0.f, 0.f};
#pragma unroll
      for (int i = 0; i < 4; ++i) {
        s1[0] = dot2(h0[i], 0x3f803f80u, s1[0]); s2[0] = dot2(h0[i], h0[i], s2[0]);
        s1[1] = dot2(h1[i], 0x3f803f80u, s1[1]); s2[1] = dot2(h1[i], h1[i], s2[1]);
        s1[2] = dot2(h2[i], 0x3f803f80u, s1[2]); s2[2] = dot2(h2[i], h2[i], s2[2]);
      }
      const f32x2 sw = swap32((s1[0] + s1[1]) + s1[2], (s2[0] + s2[1]) + s2[2]);
      const float r = sum16(sw[0] + sw[1]) * (1.f / D);
      const f32x2 mq = swap32(r, r);
      const float rden = ln_rden(vmax(fmaf(-mq[0], mq[0], mq[1]), 0.f));
      if ((lane & 47) == 0) *(LAS f32x2*)(L + L_ST + g * 8) = f32x2{rbf(mq[0]), rden};
      BAR();
    }
  } else {
    const int ww = wv - 9;
    if (ww == 2 && lane < 2) cpf[0] = __builtin_nontemporal_load((const GAS unsigned*)((lane ? vc : kc) + ((size_t)(b * H + h) * SK + (SK - W) + s * 256) * DH));
    BAR();
    asm volatile("" :: "v"(cpf[0]), "v"(cpf[1]), "v"(cpf[2]));
    if (ww == 4) {
      const v4u empty4 = {EMPTY, EMPTY, EMPTY, EMPTY};
      if (is_t1 || is_ffn) {
        const unsigned off = is_t1 ? tile_off<true>(t1, 0, lane) : tile_off<false>(f, 0, lane), step = is_t1 ? 4u * 32768u : 4u * 16384u;
#pragma unroll
        for (int i = 0; i < 5; ++i) st_sc1(rs, off + (unsigned)i * step, empty4);
      }
      if (lane < 32) st_sc1(rs, o_slot(h, lane >> 3) + (unsigned)((b * 4 + s) * 128 + (lane & 7) * 16), empty4);
      if (is_red && lane < 32) st_sc1(rs, h_copy(lane >> 2) + (unsigned)(rd * 64 + (lane & 3) * 16), empty4);
      TAGS();
      const unsigned char* pl = ws + WS_PR + (w >> 6) * 16384 + (w & 63) * 128;
      const unsigned first = time_line(pl + (idx & 1) * SIDE), second = time_line(pl + ((idx & 1) ^ 1) * SIDE);
      const unsigned vote = ((first < second ? idx : ~idx) ^ (g8 >> 2)) & 1u;
      VMWAIT(0);
      if (lane == 0) st_sc1(rs, WS_FL + (unsigned)w * 8u, v2u{((unsigned)tag & ~1u) | vote, (unsigned)(tag >> 32)});
    } else {
      TAGS();
      if (ww == 1) { flag[F_TAGLO] = (unsigned)tag; flag[F_TAGHI] = (unsigned)(tag >> 32); flag[F_TAG] = 1u; }
      lds_wait_ge(flag + F_QPUB, 1u);
      __builtin_amdgcn_s_sleep(Q_DELAY);
      const unsigned off = WS_QP + (unsigned)(((h * 2 + b) * 8 + ww * 2) * 128) * 8u + (unsigned)lane * 16u;
      v4u qa, qb;
      POLL {
        qa = ld_sc1(rs, off); qb = ld_sc1(rs, off + 1024u);
        if (__all((gbad(qa, tlo, thi) | gbad(qb, tlo, thi)) == 0u)) break;
        MEMBAR();
      }
      *(LAS f32x2*)(L + L_QP + (ww * 128 + lane * 2) * 4) = f32x2{gval(qa.x) + gval(qb.x), gval(qa.z) + gval(qb.z)};
      if (lds_ticket(L, F_QC, lane) == 3u) {
        const LAS f32x2* qp = (const LAS f32x2*)(L + L_QP) + lane;
        const f32x2 q = (qp[0] + qp[64]) + (qp[128] + qp[192]);
        *(LAS unsigned*)(L + L_Q + cs * 16 + g * 4) = cvtpk(q[0], q[1]);
        LDS_WAIT();
        flag[F_Q] = 1u;
      }
    }
    if (is_t1) {
      lds_wait_ge(flag + F_VI, 8u);
      v4u ro[8], bo[8];
      const u16* ob = wo + (size_t)(t1 * 32 + g * 2) * D + ww * 128 + cs * 8;
#pragma unroll
      for (int i = 0; i < 8; ++i) ro[i] = ld_nt(ob + (size_t)(8 * (i & 3) + (i >> 2)) * D);
      SCHED();
      const unsigned pm = side_mask(flag + F_ALL);
      unsigned off = (o_slot(h, t1 & 3) + (unsigned)(((cs >> 3) * 4 + (cs & 3)) * 128 + g * 32 + ((cs >> 2) & 1) * 16)) ^ pm;
      unsigned p_off = tile_off<true>(t1, ww, lane) ^ pm;
      asm volatile("" : "+v"(off), "+v"(p_off));
      lds_wait_ge(flag + F_OPUB, 1u);
      __builtin_amdgcn_s_sleep(O_DELAY);
      v4u oc = ld_sc1(rs, off);
      SCHED();
      tr8(ro, bo);
      PIN8(bo);
      POLL {
        if (!__any(top(oc, 0u) == EMPTY)) break;
        MEMBAR();
        oc = ld_sc1(rs, off);
      }
      const float ms = __uint_as_float(__builtin_amdgcn_perm(oc.w, oc.z, 0x04000c0cu)), lg = (float)__builtin_amdgcn_perm(oc.y, oc.x, 0x0c0c0400u) * (1.f / 8191.f);
      float M = vmax(ms, dpp<XOR1>(ms)); M = vmax(M, dpp<XOR2>(M));
      float fs = ex2(fmaf(ms - M, LOG2E, lg));
      float a0 = __uint_as_float(oc.x) * fs, a1 = __uint_as_float(oc.y) * fs, a2 = __uint_as_float(oc.z) * fs, a3 = __uint_as_float(oc.w) * fs;
      asm("" : "+v"(a0), "+v"(a1), "+v"(a2), "+v"(a3));
      fs += dpp<XOR1>(fs); a0 += dpp<XOR1>(a0); a1 += dpp<XOR1>(a1); a2 += dpp<XOR1>(a2); a3 += dpp<XOR1>(a3);
      fs += dpp<XOR2>(fs); a0 += dpp<XOR2>(a0); a1 += dpp<XOR2>(a1); a2 += dpp<XOR2>(a2); a3 += dpp<XOR2>(a3);
      const float rl = rcp(fs);
      tile_product(rs, p_off, g, join_quads(cvtpk(a0 * rl, a1 * rl), cvtpk(a2 * rl, a3 * rl)), bo);
      if (lane == 0) lds_inc(L, F_T1);
    } else if (is_ffn) {
      v4u rg[16], bg[16];
      const u16* gb = ((cs & 4) ? wu : wg) + (size_t)(ww * 128 + g * 16 + (cs >> 3) * 4) * F + f * 32 + (cs & 3) * 8;
      const bool writer = g == (cs >> 3) * 2;
      const v4u ones = {0x3f803f80u, 0x3f803f80u, 0x3f803f80u, 0x3f803f80u};
      float c0[8];
      lds_wait_ge(flag + F_VI, 8u);
      const unsigned tg = now32() + PF0 + GLAG;
      {
        wait_until(tg - PF0 - GLAG + DPF + ww * DPS);
        const unsigned dp = *(const GAS unsigned*)(wd + (size_t)f * 32 * D + (ww * 64 + lane) * 64);
        asm volatile("" :: "v"(dp));
      }
#pragma unroll
      for (int i = 0; i < 16; ++i) {
        if (i % GSTEP == 0) wait_until(tg + ((i + GSTEP - 1) * 5 * PFS) / 8);
        rg[i] = *(const GAS v4u*)(gb + (size_t)gu_row(i) * F);
        SCHED();
        if (i == 11) {
          tr8(rg, bg);
#pragma unroll
          for (int c = 0; c < 8; ++c) c0[c] = mfma32(ones, bg[c], f32x4{0.f, 0.f, 0.f, 0.f})[0];
          PIN8(c0);
          SCHED();
        }
      }
      unsigned h_off = (h_copy(g8) ^ side_mask(flag + F_ALL)) + (unsigned)((ww * 8 + (cs & 1) * 4 + (cs >> 3) * 2) * 64 + g * 16);
      unsigned gw_off = L_GU + (ww * 2 + (cs >> 3)) * 512 + ((cs >> 1) & 3) * 128 + (cs & 1) * 8;
      asm volatile("" : "+v"(h_off), "+v"(gw_off));
      tr8(rg + 8, bg + 8);
      PIN8(bg + 8);
      v4u a0 = ld_sc1(rs, h_off), a1 = ld_sc1(rs, h_off + 64u);
      SCHED();
#pragma unroll
      for (int c = 0; c < 8; ++c) c0[c] += mfma32(ones, bg[8 + c], f32x4{0.f, 0.f, 0.f, 0.f})[0];
      if (writer) {
#pragma unroll
        for (int c = 0; c < 8; ++c) *(LAS float*)(L + gw_off + (L_CS - L_GU) + c * 16) = c0[c];
      }
      if (lane == 0) lds_inc(L, F_CS);
      POLL {
        if (!__any(top(a1, top(a0, 0u)) == EMPTY)) break;
        MEMBAR();
        a0 = ld_sc1(rs, h_off); a1 = ld_sc1(rs, h_off + 64u);
      }
      f32x4 acc[8];
#pragma unroll
      for (int c = 0; c < 8; ++c) acc[c] = mfma32(a1, bg[8 + c], mfma32(a0, bg[c], f32x4{0.f, 0.f, 0.f, 0.f}));
      KEEP8(acc);
      if (writer) {
#pragma unroll
        for (int c = 0; c < 8; ++c) *(LAS f32x2*)(L + gw_off + c * 16) = f32x2{acc[c][0], acc[c][1]};
      }
      BAR();
    } else if (idx >= 8) {
      const u16* pp = ((lane & 4) ? wu : wg) + (size_t)((lane >> 4) * 16 + ((lane >> 3) & 1) * 4) * F + (lg0 + min(lane & 3, nlg - 1)) * 64;
      const int n0 = 8 * ww + idx - 8, n1 = n0 + 40;
      lds_wait_ge(flag + F_VI, 8u);
      const unsigned tp = now32() + PF0 + ww * PFS;
      wait_until(tp);
      const unsigned p0 = *(const GAS unsigned*)(pp + (size_t)((n0 % 5) * 128 + gu_row(n0 / 5)) * F);
      wait_until(tp + 5 * PFS);
      const unsigned p1 = *(const GAS unsigned*)(pp + (size_t)((n1 % 5) * 128 + gu_row(n1 / 5)) * F);
      asm volatile("" :: "v"(p0), "v"(p1));
    }
  }

  if (is_red && wv >= 1 && wv <= 8) {
    const int col = ((rd >> 2) & 1) * D + (rd >> 3) * 128 + (rd & 1) * 64 + (lane & 7) * 8 + ((rd >> 1) & 1) * 4 + (lane >> 4);
    const unsigned xv = x[col];
    const unsigned pm = side_mask(flag + F_ALL);
    unsigned hb_off = (h_copy(lane >> 3) + (unsigned)(rd * 64 + (lane & 7) * 8)) ^ pm;
    u16* op = out + col;
    asm volatile("" : "+v"(hb_off), "+v"(op));
    lds_wait_ge(flag + F_T1, 5u);
    __builtin_amdgcn_s_sleep(T2_DELAY);
    const float hs = lo2f(xv) + rbf(reduce_inbox<128>(rs, inbox_off<true>(rd) ^ pm, wv - 1, lane, L, L_RED));
    const unsigned hb = cvtpk(hs, hs);
    if (wv == 1) {
      const auto sw = __builtin_amdgcn_permlane16_swap(hb, hb, false, false);
      const unsigned hp = min(__builtin_amdgcn_perm(sw[1], sw[0], 0x05040100u), EMPTY - 1u);
      const auto sv = __builtin_amdgcn_permlane32_swap(hp, hp, false, false);
      st_sc1(rs, hb_off, v2u{sv[0], sv[1]});
    }
    if (rd == 0 && wv == 8) { const u64 e1 = load_epoch(ws); if (lane == 0) st_sc1(rs, WS_EPOCH, v2u{(unsigned)e1, (unsigned)(e1 >> 32)}); }
    __builtin_amdgcn_s_sleep(T4_DELAY);
    const float tot = reduce_inbox<NFFN>(rs, inbox_off<false>(rd) ^ pm, wv - 1, lane, L, L_RED + 2048);
    if (wv == 2) {
      if ((lane & 8) == 0) *(GAS u16*)op = (u16)cvtpk(lo2f(hb) + rbf(tot), 0.f);
    }
  }
}

extern "C" void kernel_launch(void* const* d_in, const int* in_sizes, int n_in, void* d_out, int out_size, void* d_ws, size_t ws_size, hipStream_t stream) {
  fused_block<<<NWG, NTHR, 0, stream>>>((const u16*)d_in[0], (const u16*)d_in[1], (const u16*)d_in[2], (const u16*)d_in[3], (const u16*)d_in[4],
                                        (const u16*)d_in[6], (const u16*)d_in[7], (const u16*)d_in[8], (u16*)d_out, (unsigned char*)d_ws);
}
```
